```python
import torch

def _softmax(x, axis=-1):
    return torch.softmax(x, dim=axis)
def _gelu(x):
    return torch.nn.functional.gelu(x, approximate="tanh")
def _silu(x):
    return torch.nn.functional.silu(x)
def _sigmoid(x):
    return torch.sigmoid(x)
def _layernorm(x, axis=-1, eps=1e-5):
    m = x.mean(dim=axis, keepdim=True)
    v = x.var(dim=axis, keepdim=True, unbiased=False)
    return (x - m) / torch.sqrt(v + eps)
def _scatter_add(tgt, idx, src, axis=0):
    return tgt.clone().index_add_(axis, idx, src)
def _segment_sum(src, seg, n):
    out = torch.zeros((n,) + src.shape[1:], dtype=src.dtype)
    return out.index_add_(0, seg, src)
def _pad(x, pw):
    flat = tuple(v for pair in reversed(pw) for v in pair)
    return torch.nn.functional.pad(x, flat)

kernel_name = 'standalone__gqa_block_cached__near_ridge'

def setup_inputs():

    _w = (lambda a: (lambda t: t.to(torch.bfloat16) if t.is_floating_point() else t)(torch.as_tensor(a)))
    B, Sq, D = 8, 256, 512
    Hq, Hkv, Sk, Dh = 32, 32, 1024, 128
    assert Hq % Hkv == 0, (Hq, Hkv)
    return {
        "x": _w((lambda *s: torch.randn(*s, dtype=torch.bfloat16) * 0.8)(B, Sq, D)),
        "k_cache": _w((lambda *s: torch.randn(*s, dtype=torch.bfloat16) * 0.8)(B, Hkv, Sk, Dh)),
        "v_cache": _w((lambda *s: torch.randn(*s, dtype=torch.bfloat16) * 0.05)(B, Hkv, Sk, Dh)),
        "wq": _w((lambda *s: torch.randn(*s, dtype=torch.bfloat16) * 0.8)(D, Hq * Dh) * (D ** -0.5)),
        "wo": _w((lambda *s: torch.randn(*s, dtype=torch.bfloat16) * 0.05)(Hq * Dh, D) * ((Hq * Dh) ** -0.5)),
    }

def reference(x, k_cache, v_cache, wq, wo):
    B, Sq, D = x.shape
    Hq, Dh = 32, 128
    Hkv = k_cache.shape[1]
    rep = Hq // Hkv
    q = (lambda x, *d: x.permute(*d))((x @ wq).reshape(B, Sq, Hq, Dh), 0, 2, 1, 3)
    k = (lambda x, n, axis: torch.repeat_interleave(x, n, dim=axis))(k_cache, rep, axis=1)
    v = (lambda x, n, axis: torch.repeat_interleave(x, n, dim=axis))(v_cache, rep, axis=1)
    scores = torch.einsum("bhqd,bhkd->bhqk", q, k) * (Dh ** -0.5)
    p = _softmax(scores, axis=-1)
    o = torch.einsum("bhqk,bhkd->bhqd", p, v)
    return (lambda x, *d: x.permute(*d))(o, 0, 2, 1, 3).reshape(B, Sq, Hq * Dh) @ wo
```

```cpp
#include <hip/hip_runtime.h>
#include <hip/hip_bf16.h>

using bf16 = __hip_bfloat16;
constexpr int   D = 128, NW = 8, QBLK = 32, KVBLK = 64;
constexpr float SCALE = 0.088388347648318440f;
constexpr float THR = 8.f;
constexpr int SDEPTH = 2;
constexpr int B = 8, HQ = 32, S = 256, SKV = 1024, DM = 512;
constexpr int LDQ = HQ * D, LDK = D, LDO = HQ * D;
constexpr size_t SHM_V = KVBLK * D * 2, SHM_K = KVBLK * D * 2, SHM_ATTN = 2 * SHM_V + 2 * SHM_K + NW * 64 * 4;

using bf16x8 = __attribute__((ext_vector_type(8))) short;
using s16x4  = __attribute__((ext_vector_type(4))) short;
using f32x16 = __attribute__((ext_vector_type(16))) float;
using f32x8  = __attribute__((ext_vector_type(8))) float;
using u32x4  = __attribute__((ext_vector_type(4))) unsigned;
#define KSWZ(row, colB) ((row) * 256 + ((colB) ^ (((row) & 7) << 4)))
#define SBAR() __builtin_amdgcn_sched_barrier(0)
__device__ __forceinline__ int crow(int r, int hi) { return (r & 3) + 8 * (r >> 2) + 4 * hi; }
__device__ __forceinline__ unsigned cvtpk(float lo, float hi) {
  unsigned r; asm volatile("v_cvt_pk_bf16_f32 %0, %1, %2" : "=v"(r) : "v"(lo), "v"(hi)); return r;
}
__device__ __forceinline__ bf16x8 ld8(const bf16* p) { return *reinterpret_cast<const bf16x8*>(p); }

__device__ __forceinline__ void partialSM(f32x16& p0, f32x16& p1, float& m_reg, float& mn, float& alpha) {
  constexpr float C = SCALE * 1.4426950408889634f;
  float pmax = p0[0]; for (int r = 1; r < 16; ++r) pmax = fmaxf(pmax, p0[r]); for (int r = 0; r < 16; ++r) pmax = fmaxf(pmax, p1[r]);
  { auto rr = __builtin_amdgcn_permlane32_swap(__float_as_uint(pmax), __float_as_uint(pmax), false, false);
    pmax = fmaxf(__uint_as_float(rr[0]), __uint_as_float(rr[1])); }
  if (__builtin_expect(__all(pmax - m_reg <= THR / SCALE), 1)) { mn = m_reg; alpha = 1.f; }
  else { mn = fmaxf(m_reg, pmax); alpha = __builtin_amdgcn_exp2f((m_reg - mn) * C); m_reg = mn; }
  float mnC = -mn * C;
  for (int r = 0; r < 16; ++r) p0[r] = fmaf(p0[r], C, mnC); for (int r = 0; r < 16; ++r) p1[r] = fmaf(p1[r], C, mnC);
  for (int r = 0; r < 16; ++r) p0[r] = __builtin_amdgcn_exp2f(p0[r]);
}
__device__ __forceinline__ void finishSM(f32x16& p0, f32x16& p1, float alpha, float& l_reg, bf16x8& pa0, bf16x8& pa1, bf16x8& pa2, bf16x8& pa3) {
  for (int r = 0; r < 16; ++r) p1[r] = __builtin_amdgcn_exp2f(p1[r]);
  float ps = 0; for (int r = 0; r < 16; ++r) ps += p0[r]; for (int r = 0; r < 16; ++r) ps += p1[r];
  { auto rr = __builtin_amdgcn_permlane32_swap(__float_as_uint(ps), __float_as_uint(ps), false, false);
    ps = __uint_as_float(rr[0]) + __uint_as_float(rr[1]); }
  l_reg = l_reg * alpha + ps;
#define PK4(P, BASE, OUT) do { unsigned a0 = cvtpk(P[BASE + 0], P[BASE + 1]), a1 = cvtpk(P[BASE + 2], P[BASE + 3]);   \
    unsigned b0 = cvtpk(P[BASE + 4], P[BASE + 5]), b1 = cvtpk(P[BASE + 6], P[BASE + 7]);                              \
    auto r0 = __builtin_amdgcn_permlane32_swap(a0, b0, false, false); auto r1 = __builtin_amdgcn_permlane32_swap(a1, b1, false, false); \
    u32x4 w = {r0[0], r1[0], r0[1], r1[1]}; OUT = *reinterpret_cast<bf16x8*>(&w); } while (0)
  PK4(p0, 0, pa0); PK4(p0, 8, pa1); PK4(p1, 0, pa2); PK4(p1, 8, pa3);
#undef PK4
}
__device__ __forceinline__ void qkt(f32x16& p0, f32x16& p1, const bf16* Ks, const bf16x8* qr, int r32, int hi) {
  p0 = f32x16{}; p1 = f32x16{};
  for (int d0 = 0; d0 < 8; ++d0) { int cb = (d0 * 16 + hi * 8) * 2;
    bf16x8 b0 = *reinterpret_cast<const bf16x8*>((const char*)Ks + KSWZ(r32, cb));
    bf16x8 b1 = *reinterpret_cast<const bf16x8*>((const char*)Ks + KSWZ(32 + r32, cb));
    p0 = __builtin_amdgcn_mfma_f32_32x32x16_bf16(b0, qr[d0], p0, 0, 0, 0);
    p1 = __builtin_amdgcn_mfma_f32_32x32x16_bf16(b1, qr[d0], p1, 0, 0, 0); }
}
__device__ __forceinline__ int v_st(int k, int c) { const int kk = (k & ~0xC) | ((k & 4) << 1) | ((k & 8) >> 1); return ((kk >> 3) * 4 + (c >> 5)) * 512 + ((kk & 7) * 32 + (c & 31)) * 2; }
__device__ __forceinline__ int v_rd_base(int lane) { return ((lane & 3) << 3) | (((lane >> 2) & 3) << 6) | (((lane >> 4) & 1) << 5) | (((lane >> 5) & 1) << 8); }
constexpr int v_rd_off(int d0, int ks, int half) { return d0 * 512 + ks * 4096 + half * 2048; }
template <int OFF> __device__ __forceinline__ s16x4 tr_read(int vb) {
  s16x4 r; asm volatile("ds_read_b64_tr_b16 %0, %1 offset:%2" : "=&v"(r) : "v"(vb), "i"(OFF) : "memory"); return r;
}
template <int D0> __device__ __forceinline__ void pv_one(f32x16& od, int vb, bf16x8 pa0, bf16x8 pa1, bf16x8 pa2, bf16x8 pa3) {
  const s16x4 l0 = tr_read<v_rd_off(D0, 0, 0)>(vb), h0 = tr_read<v_rd_off(D0, 0, 1)>(vb), l1 = tr_read<v_rd_off(D0, 1, 0)>(vb), h1 = tr_read<v_rd_off(D0, 1, 1)>(vb);
  const s16x4 l2 = tr_read<v_rd_off(D0, 2, 0)>(vb), h2 = tr_read<v_rd_off(D0, 2, 1)>(vb), l3 = tr_read<v_rd_off(D0, 3, 0)>(vb), h3 = tr_read<v_rd_off(D0, 3, 1)>(vb);
  asm volatile("s_waitcnt lgkmcnt(0)" ::: "memory"); SBAR();
#define PK(L, H) (bf16x8){L[0], L[1], L[2], L[3], H[0], H[1], H[2], H[3]}
  od = __builtin_amdgcn_mfma_f32_32x32x16_bf16(pa0, PK(l0, h0), od, 0, 0, 0);
  od = __builtin_amdgcn_mfma_f32_32x32x16_bf16(pa1, PK(l1, h1), od, 0, 0, 0);
  od = __builtin_amdgcn_mfma_f32_32x32x16_bf16(pa2, PK(l2, h2), od, 0, 0, 0);
  od = __builtin_amdgcn_mfma_f32_32x32x16_bf16(pa3, PK(l3, h3), od, 0, 0, 0);
#undef PK
}
__device__ __forceinline__ void pv_d0(f32x16* o, int vb, bf16x8 pa0, bf16x8 pa1, bf16x8 pa2, bf16x8 pa3) {
  pv_one<0>(o[0], vb, pa0, pa1, pa2, pa3); pv_one<1>(o[1], vb, pa0, pa1, pa2, pa3); pv_one<2>(o[2], vb, pa0, pa1, pa2, pa3); pv_one<3>(o[3], vb, pa0, pa1, pa2, pa3);
}

__device__ __forceinline__ void attn_dense_body(const bf16* __restrict__ Qb, const bf16* __restrict__ Kh, const bf16* __restrict__ Vh,
                                                bf16* __restrict__ Ob, int seq, char* lds) {
  const int tid = threadIdx.x, wid = tid >> 6, lane = tid & 63, r32 = lane & 31, hi = lane >> 5;
  bf16* V_lds = (bf16*)lds; bf16* K_lds = (bf16*)(lds + 2 * SHM_V);
  float* ws = (float*)(lds + 2 * SHM_V + 2 * SHM_K) + wid * 64; float* li_l = ws; float* al_l = ws + 32;
  float m_reg = -1e30f, l_reg = 0; f32x16 o[4] = {}; bf16x8 qr[8];
  const bf16* Qw = Qb + (long)(wid * QBLK + r32) * LDQ + hi * 8;
#pragma unroll
  for (int d0 = 0; d0 < 8; ++d0) qr[d0] = ld8(Qw + d0 * 16);
  const int sr = tid >> 4, sc = (tid & 15) * 8, vst0 = v_st(sr, sc), vst1 = v_st(32 + sr, sc);
  const int vb0 = (int)(uintptr_t)V_lds + v_rd_base(lane);
  struct { bf16x8 vs0, vs1, ks0, ks1; } sr_[SDEPTH];
#define SLOAD(i, k0) do { sr_[i].vs0 = ld8(&Vh[(long)((k0) + sr) * LDK + sc]); sr_[i].vs1 = ld8(&Vh[(long)((k0) + 32 + sr) * LDK + sc]); \
    sr_[i].ks0 = ld8(&Kh[(long)((k0) + sr) * LDK + sc]); sr_[i].ks1 = ld8(&Kh[(long)((k0) + 32 + sr) * LDK + sc]); } while (0)
#define SWRITE(b, i) do { *(bf16x8*)((char*)V_lds + (b) * SHM_V + vst0) = sr_[i].vs0;          \
    *(bf16x8*)((char*)V_lds + (b) * SHM_V + vst1) = sr_[i].vs1; int kc = sc * 2;               \
    *(bf16x8*)((char*)K_lds + (b) * SHM_K + KSWZ(sr, kc)) = sr_[i].ks0;                       \
    *(bf16x8*)((char*)K_lds + (b) * SHM_K + KSWZ(32 + sr, kc)) = sr_[i].ks1; } while (0)
#define SWAIT() do { asm volatile("s_waitcnt vmcnt(4)" ::: "memory"); } while (0)
#define RESC(a) do { if (__any((a) < 1.f)) { if (hi == 0) al_l[r32] = (a); asm volatile("s_waitcnt lgkmcnt(0)" ::: "memory"); \
    for (int d = 0; d < 4; ++d) for (int r = 0; r < 16; ++r) o[d][r] *= al_l[crow(r, hi)]; } } while (0)
  f32x16 pA0, pA1, pB0, pB1; float mnA, mnB, alA, alB; bf16x8 pa0, pa1, pa2, pa3; const int NT = seq / KVBLK;
  constexpr int SE = 0, SO = SDEPTH - 1;
  SLOAD(SE, 0); asm volatile("s_waitcnt vmcnt(0)" ::: "memory"); SWRITE(0, SE); __syncthreads();
  qkt(pA0, pA1, K_lds, qr, r32, hi); partialSM(pA0, pA1, m_reg, mnA, alA);
  SLOAD(SO, KVBLK); if (2 < NT) SLOAD(SE, 2 * KVBLK);
  SWAIT(); SWRITE(1, SO); __syncthreads();
  for (int j = 1; j + 1 < NT; j += 2) {
    SBAR(); qkt(pB0, pB1, (bf16*)((char*)K_lds + SHM_K), qr, r32, hi);
    finishSM(pA0, pA1, alA, l_reg, pa0, pa1, pa2, pa3); SBAR();
    SLOAD(SO, (j + SDEPTH) * KVBLK); SBAR();
    pv_d0(o, vb0, pa0, pa1, pa2, pa3); partialSM(pB0, pB1, m_reg, mnB, alB);
    __syncthreads(); SWAIT(); SWRITE(0, SE);
    RESC(alB); __syncthreads();
    SBAR(); qkt(pA0, pA1, K_lds, qr, r32, hi);
    finishSM(pB0, pB1, alB, l_reg, pa0, pa1, pa2, pa3); SBAR();
    if (j + 3 < NT) SLOAD(SE, (j + 1 + SDEPTH) * KVBLK); SBAR();
    pv_d0(o, vb0 + (int)SHM_V, pa0, pa1, pa2, pa3); partialSM(pA0, pA1, m_reg, mnA, alA);
    __syncthreads(); SWAIT(); SWRITE(1, SO);
    RESC(alA); __syncthreads();
  }
  SBAR(); qkt(pB0, pB1, (bf16*)((char*)K_lds + SHM_K), qr, r32, hi);
  finishSM(pA0, pA1, alA, l_reg, pa0, pa1, pa2, pa3); SBAR();
  pv_d0(o, vb0, pa0, pa1, pa2, pa3); partialSM(pB0, pB1, m_reg, mnB, alB);
  __syncthreads(); RESC(alB);
  finishSM(pB0, pB1, alB, l_reg, pa0, pa1, pa2, pa3); SBAR();
  pv_d0(o, vb0 + (int)SHM_V, pa0, pa1, pa2, pa3);
  if (hi == 0) li_l[r32] = l_reg; asm volatile("s_waitcnt lgkmcnt(0)" ::: "memory");
  float rli[16];
#pragma unroll
  for (int r = 0; r < 16; ++r) rli[r] = __builtin_amdgcn_rcpf(li_l[crow(r, hi)]);
  bf16* Ow = Ob + (long)(wid * QBLK) * LDO;
#pragma unroll
  for (int r = 0; r < 16; ++r) { int orow = crow(r, hi);
    for (int d0 = 0; d0 < 4; ++d0) Ow[(long)orow * LDO + d0 * 32 + r32] = __float2bfloat16(o[d0][r] * rli[r]); }
}

__global__ __launch_bounds__(64 * NW, 1)
void attn_fwd_dense(const bf16* __restrict__ Q, const bf16* __restrict__ K, const bf16* __restrict__ V, bf16* __restrict__ O) {
  extern __shared__ __attribute__((aligned(16))) char shm[];
  const long b = blockIdx.z, h = blockIdx.y;
  const long q0 = ((b * S) * HQ + h) * (long)D, k0 = (b * HQ + h) * SKV * (long)D;
  attn_dense_body(Q + q0, K + k0, V + k0, O + q0, SKV, shm);
}

__global__ __launch_bounds__(256) void gemm_simple(const bf16* __restrict__ A, const bf16* __restrict__ Bm, bf16* __restrict__ C, int M, int N, int K) {
  const int wid = threadIdx.x >> 6, lane = threadIdx.x & 63, r = lane & 31, h = lane >> 5;
  const int m0 = blockIdx.y * 64 + (wid >> 1) * 32, n0 = blockIdx.x * 64 + (wid & 1) * 32;
  f32x16 acc = {};
  const unsigned short* Bs = (const unsigned short*)Bm;
  for (int k0 = 0; k0 < K; k0 += 16) {
    bf16x8 a = ld8(A + (long)(m0 + r) * K + k0 + 8 * h);
    bf16x8 b;
#pragma unroll
    for (int j = 0; j < 8; ++j) b[j] = (short)Bs[(long)(k0 + 8 * h + j) * N + n0 + r];
    acc = __builtin_amdgcn_mfma_f32_32x32x16_bf16(a, b, acc, 0, 0, 0);
  }
#pragma unroll
  for (int i = 0; i < 16; ++i) C[(long)(m0 + crow(i, h)) * N + n0 + r] = __float2bfloat16(acc[i]);
}

extern "C" void kernel_launch(void* const* d_in, const int* in_sizes, int n_in,
                              void* d_out, int out_size, void* d_ws, size_t ws_size,
                              hipStream_t stream) {
  const bf16* x = (const bf16*)d_in[0];
  const bf16* kc = (const bf16*)d_in[1];
  const bf16* vc = (const bf16*)d_in[2];
  const bf16* wq = (const bf16*)d_in[3];
  const bf16* wo = (const bf16*)d_in[4];
  bf16* q = (bf16*)d_ws;
  bf16* o = q + (size_t)B * S * HQ * D;
  hipFuncSetAttribute((const void*)attn_fwd_dense, hipFuncAttributeMaxDynamicSharedMemorySize, (int)SHM_ATTN);
  gemm_simple<<<dim3(HQ * D / 64, B * S / 64), 256, 0, stream>>>(x, wq, q, B * S, HQ * D, DM);
  attn_fwd_dense<<<dim3(1, HQ, B), 64 * NW, SHM_ATTN, stream>>>(q, kc, vc, o);
  gemm_simple<<<dim3(DM / 64, B * S / 64), 256, 0, stream>>>(o, wo, (bf16*)d_out, B * S, DM, HQ * D);
}
```

```cpp
#include <hip/hip_runtime.h>
#include <hip/hip_bf16.h>
#include <utility>

constexpr int D = 128, NT = 16;
constexpr unsigned KRS = 256u, KTILEB = 64u * KRS;
constexpr size_t SHM_ATTN = 163840;
typedef float    f32x16 __attribute__((ext_vector_type(16)));
typedef float    f32x2  __attribute__((ext_vector_type(2)));
typedef float    f32x4  __attribute__((ext_vector_type(4)));
typedef __bf16   bf16x8 __attribute__((ext_vector_type(8)));
typedef __bf16   bf16x4 __attribute__((ext_vector_type(4)));
typedef __bf16   bf16x2 __attribute__((ext_vector_type(2)));
typedef unsigned u32x4  __attribute__((ext_vector_type(4)));
typedef unsigned u32x2  __attribute__((ext_vector_type(2)));
template <int... Is> using iseq = std::integer_sequence<int, Is...>;
template <int N> using make_iseq = std::make_integer_sequence<int, N>;
#define KVPOL " nt"
#define STAMP2_ASM "s_nop 0"
#define KRING 0x00000u
#define VRING 0x10000u
#define OSTAGE 0x20000u
#define SRD_WORD3 0x00020000
#define DI __device__ __forceinline__
#define GAP() __builtin_amdgcn_sched_barrier(0)

#define W4_ACLOB_HI "a128","a129","a130","a131","a132","a133","a134","a135","a136","a137","a138","a139","a140","a141","a142","a143","a144","a145","a146","a147","a148","a149","a150","a151","a152","a153","a154","a155","a156","a157","a158","a159","a160","a161","a162","a163","a164","a165","a166","a167","a168","a169","a170","a171","a172","a173","a174","a175","a176","a177","a178","a179","a180","a181","a182","a183","a184","a185","a186","a187","a188","a189","a190","a191","a192","a193","a194","a195","a196","a197","a198","a199","a200","a201","a202","a203","a204","a205","a206","a207","a208","a209","a210","a211","a212","a213","a214","a215","a216","a217","a218","a219","a220","a221","a222","a223","a224","a225","a226","a227","a228","a229","a230","a231","a232","a233","a234","a235","a236","a237","a238","a239","a240","a241","a242","a243","a244","a245","a246","a247","a248","a249","a250","a251","a252","a253","a254","a255"
#define W4_ACLOB "a0","a1","a2","a3","a4","a5","a6","a7","a8","a9","a10","a11","a12","a13","a14","a15","a16","a17","a18","a19","a20","a21","a22","a23","a24","a25","a26","a27","a28","a29","a30","a31","a32","a33","a34","a35","a36","a37","a38","a39","a40","a41","a42","a43","a44","a45","a46","a47","a48","a49","a50","a51","a52","a53","a54","a55","a56","a57","a58","a59","a60","a61","a62","a63","a64","a65","a66","a67","a68","a69","a70","a71","a72","a73","a74","a75","a76","a77","a78","a79","a80","a81","a82","a83","a84","a85","a86","a87","a88","a89","a90","a91","a92","a93","a94","a95","a96","a97","a98","a99","a100","a101","a102","a103","a104","a105","a106","a107","a108","a109","a110","a111","a112","a113","a114","a115","a116","a117","a118","a119","a120","a121","a122","a123","a124","a125","a126","a127"
#define W4_READ_O(DB, QB, o) do { switch (2 * (DB) + (QB)) { \
    case 0: asm volatile("v_accvgpr_read_b32 %0, a0\n\t" "v_accvgpr_read_b32 %1, a1\n\t" "v_accvgpr_read_b32 %2, a2\n\t" "v_accvgpr_read_b32 %3, a3\n\t" "v_accvgpr_read_b32 %4, a4\n\t" "v_accvgpr_read_b32 %5, a5\n\t" "v_accvgpr_read_b32 %6, a6\n\t" "v_accvgpr_read_b32 %7, a7\n\t" "v_accvgpr_read_b32 %8, a8\n\t" "v_accvgpr_read_b32 %9, a9\n\t" "v_accvgpr_read_b32 %10, a10\n\t" "v_accvgpr_read_b32 %11, a11\n\t" "v_accvgpr_read_b32 %12, a12\n\t" "v_accvgpr_read_b32 %13, a13\n\t" "v_accvgpr_read_b32 %14, a14\n\t" "v_accvgpr_read_b32 %15, a15\n\t" : "=v"((o)[0]), "=v"((o)[1]), "=v"((o)[2]), "=v"((o)[3]), "=v"((o)[4]), "=v"((o)[5]), "=v"((o)[6]), "=v"((o)[7]), "=v"((o)[8]), "=v"((o)[9]), "=v"((o)[10]), "=v"((o)[11]), "=v"((o)[12]), "=v"((o)[13]), "=v"((o)[14]), "=v"((o)[15])); break; \
    case 1: asm volatile("v_accvgpr_read_b32 %0, a16\n\t" "v_accvgpr_read_b32 %1, a17\n\t" "v_accvgpr_read_b32 %2, a18\n\t" "v_accvgpr_read_b32 %3, a19\n\t" "v_accvgpr_read_b32 %4, a20\n\t" "v_accvgpr_read_b32 %5, a21\n\t" "v_accvgpr_read_b32 %6, a22\n\t" "v_accvgpr_read_b32 %7, a23\n\t" "v_accvgpr_read_b32 %8, a24\n\t" "v_accvgpr_read_b32 %9, a25\n\t" "v_accvgpr_read_b32 %10, a26\n\t" "v_accvgpr_read_b32 %11, a27\n\t" "v_accvgpr_read_b32 %12, a28\n\t" "v_accvgpr_read_b32 %13, a29\n\t" "v_accvgpr_read_b32 %14, a30\n\t" "v_accvgpr_read_b32 %15, a31\n\t" : "=v"((o)[0]), "=v"((o)[1]), "=v"((o)[2]), "=v"((o)[3]), "=v"((o)[4]), "=v"((o)[5]), "=v"((o)[6]), "=v"((o)[7]), "=v"((o)[8]), "=v"((o)[9]), "=v"((o)[10]), "=v"((o)[11]), "=v"((o)[12]), "=v"((o)[13]), "=v"((o)[14]), "=v"((o)[15])); break; \
    case 2: asm volatile("v_accvgpr_read_b32 %0, a32\n\t" "v_accvgpr_read_b32 %1, a33\n\t" "v_accvgpr_read_b32 %2, a34\n\t" "v_accvgpr_read_b32 %3, a35\n\t" "v_accvgpr_read_b32 %4, a36\n\t" "v_accvgpr_read_b32 %5, a37\n\t" "v_accvgpr_read_b32 %6, a38\n\t" "v_accvgpr_read_b32 %7, a39\n\t" "v_accvgpr_read_b32 %8, a40\n\t" "v_accvgpr_read_b32 %9, a41\n\t" "v_accvgpr_read_b32 %10, a42\n\t" "v_accvgpr_read_b32 %11, a43\n\t" "v_accvgpr_read_b32 %12, a44\n\t" "v_accvgpr_read_b32 %13, a45\n\t" "v_accvgpr_read_b32 %14, a46\n\t" "v_accvgpr_read_b32 %15, a47\n\t" : "=v"((o)[0]), "=v"((o)[1]), "=v"((o)[2]), "=v"((o)[3]), "=v"((o)[4]), "=v"((o)[5]), "=v"((o)[6]), "=v"((o)[7]), "=v"((o)[8]), "=v"((o)[9]), "=v"((o)[10]), "=v"((o)[11]), "=v"((o)[12]), "=v"((o)[13]), "=v"((o)[14]), "=v"((o)[15])); break; \
    case 3: asm volatile("v_accvgpr_read_b32 %0, a48\n\t" "v_accvgpr_read_b32 %1, a49\n\t" "v_accvgpr_read_b32 %2, a50\n\t" "v_accvgpr_read_b32 %3, a51\n\t" "v_accvgpr_read_b32 %4, a52\n\t" "v_accvgpr_read_b32 %5, a53\n\t" "v_accvgpr_read_b32 %6, a54\n\t" "v_accvgpr_read_b32 %7, a55\n\t" "v_accvgpr_read_b32 %8, a56\n\t" "v_accvgpr_read_b32 %9, a57\n\t" "v_accvgpr_read_b32 %10, a58\n\t" "v_accvgpr_read_b32 %11, a59\n\t" "v_accvgpr_read_b32 %12, a60\n\t" "v_accvgpr_read_b32 %13, a61\n\t" "v_accvgpr_read_b32 %14, a62\n\t" "v_accvgpr_read_b32 %15, a63\n\t" : "=v"((o)[0]), "=v"((o)[1]), "=v"((o)[2]), "=v"((o)[3]), "=v"((o)[4]), "=v"((o)[5]), "=v"((o)[6]), "=v"((o)[7]), "=v"((o)[8]), "=v"((o)[9]), "=v"((o)[10]), "=v"((o)[11]), "=v"((o)[12]), "=v"((o)[13]), "=v"((o)[14]), "=v"((o)[15])); break; \
    case 4: asm volatile("v_accvgpr_read_b32 %0, a64\n\t" "v_accvgpr_read_b32 %1, a65\n\t" "v_accvgpr_read_b32 %2, a66\n\t" "v_accvgpr_read_b32 %3, a67\n\t" "v_accvgpr_read_b32 %4, a68\n\t" "v_accvgpr_read_b32 %5, a69\n\t" "v_accvgpr_read_b32 %6, a70\n\t" "v_accvgpr_read_b32 %7, a71\n\t" "v_accvgpr_read_b32 %8, a72\n\t" "v_accvgpr_read_b32 %9, a73\n\t" "v_accvgpr_read_b32 %10, a74\n\t" "v_accvgpr_read_b32 %11, a75\n\t" "v_accvgpr_read_b32 %12, a76\n\t" "v_accvgpr_read_b32 %13, a77\n\t" "v_accvgpr_read_b32 %14, a78\n\t" "v_accvgpr_read_b32 %15, a79\n\t" : "=v"((o)[0]), "=v"((o)[1]), "=v"((o)[2]), "=v"((o)[3]), "=v"((o)[4]), "=v"((o)[5]), "=v"((o)[6]), "=v"((o)[7]), "=v"((o)[8]), "=v"((o)[9]), "=v"((o)[10]), "=v"((o)[11]), "=v"((o)[12]), "=v"((o)[13]), "=v"((o)[14]), "=v"((o)[15])); break; \
    case 5: asm volatile("v_accvgpr_read_b32 %0, a80\n\t" "v_accvgpr_read_b32 %1, a81\n\t" "v_accvgpr_read_b32 %2, a82\n\t" "v_accvgpr_read_b32 %3, a83\n\t" "v_accvgpr_read_b32 %4, a84\n\t" "v_accvgpr_read_b32 %5, a85\n\t" "v_accvgpr_read_b32 %6, a86\n\t" "v_accvgpr_read_b32 %7, a87\n\t" "v_accvgpr_read_b32 %8, a88\n\t" "v_accvgpr_read_b32 %9, a89\n\t" "v_accvgpr_read_b32 %10, a90\n\t" "v_accvgpr_read_b32 %11, a91\n\t" "v_accvgpr_read_b32 %12, a92\n\t" "v_accvgpr_read_b32 %13, a93\n\t" "v_accvgpr_read_b32 %14, a94\n\t" "v_accvgpr_read_b32 %15, a95\n\t" : "=v"((o)[0]), "=v"((o)[1]), "=v"((o)[2]), "=v"((o)[3]), "=v"((o)[4]), "=v"((o)[5]), "=v"((o)[6]), "=v"((o)[7]), "=v"((o)[8]), "=v"((o)[9]), "=v"((o)[10]), "=v"((o)[11]), "=v"((o)[12]), "=v"((o)[13]), "=v"((o)[14]), "=v"((o)[15])); break; \
    case 6: asm volatile("v_accvgpr_read_b32 %0, a96\n\t" "v_accvgpr_read_b32 %1, a97\n\t" "v_accvgpr_read_b32 %2, a98\n\t" "v_accvgpr_read_b32 %3, a99\n\t" "v_accvgpr_read_b32 %4, a100\n\t" "v_accvgpr_read_b32 %5, a101\n\t" "v_accvgpr_read_b32 %6, a102\n\t" "v_accvgpr_read_b32 %7, a103\n\t" "v_accvgpr_read_b32 %8, a104\n\t" "v_accvgpr_read_b32 %9, a105\n\t" "v_accvgpr_read_b32 %10, a106\n\t" "v_accvgpr_read_b32 %11, a107\n\t" "v_accvgpr_read_b32 %12, a108\n\t" "v_accvgpr_read_b32 %13, a109\n\t" "v_accvgpr_read_b32 %14, a110\n\t" "v_accvgpr_read_b32 %15, a111\n\t" : "=v"((o)[0]), "=v"((o)[1]), "=v"((o)[2]), "=v"((o)[3]), "=v"((o)[4]), "=v"((o)[5]), "=v"((o)[6]), "=v"((o)[7]), "=v"((o)[8]), "=v"((o)[9]), "=v"((o)[10]), "=v"((o)[11]), "=v"((o)[12]), "=v"((o)[13]), "=v"((o)[14]), "=v"((o)[15])); break; \
    case 7: asm volatile("v_accvgpr_read_b32 %0, a112\n\t" "v_accvgpr_read_b32 %1, a113\n\t" "v_accvgpr_read_b32 %2, a114\n\t" "v_accvgpr_read_b32 %3, a115\n\t" "v_accvgpr_read_b32 %4, a116\n\t" "v_accvgpr_read_b32 %5, a117\n\t" "v_accvgpr_read_b32 %6, a118\n\t" "v_accvgpr_read_b32 %7, a119\n\t" "v_accvgpr_read_b32 %8, a120\n\t" "v_accvgpr_read_b32 %9, a121\n\t" "v_accvgpr_read_b32 %10, a122\n\t" "v_accvgpr_read_b32 %11, a123\n\t" "v_accvgpr_read_b32 %12, a124\n\t" "v_accvgpr_read_b32 %13, a125\n\t" "v_accvgpr_read_b32 %14, a126\n\t" "v_accvgpr_read_b32 %15, a127\n\t" : "=v"((o)[0]), "=v"((o)[1]), "=v"((o)[2]), "=v"((o)[3]), "=v"((o)[4]), "=v"((o)[5]), "=v"((o)[6]), "=v"((o)[7]), "=v"((o)[8]), "=v"((o)[9]), "=v"((o)[10]), "=v"((o)[11]), "=v"((o)[12]), "=v"((o)[13]), "=v"((o)[14]), "=v"((o)[15])); break; \
    } } while (0)

DI unsigned rfl(unsigned x) { return __builtin_amdgcn_readfirstlane(x); }
DI unsigned cvtpk(float lo, float hi) {
  f32x2 v = {lo, hi}; bf16x2 b = __builtin_convertvector(v, bf16x2); return __builtin_bit_cast(unsigned, b);
}
DI float fmul_s(float a, float b) { float r; asm("v_mul_f32_e32 %0, %1, %2" : "=v"(r) : "v"(a), "v"(b)); return r; }

struct W4C {
  unsigned vkb[4], vvb, vksrc[2], vvsrc, m0k, m0v, sofk, sofv, ldsb;
  __amdgpu_buffer_rsrc_t srdK, srdV;
  float lsum[2];
};
constexpr int AQ_(int qb, int d0) { return 128 + 32 * qb + 4 * d0; }
constexpr int AK_(int kb, int d0) { return 192 + 32 * kb + 4 * d0; }

template <int I> DI void k_read(W4C& c, unsigned slot) {
  constexpr int kb = I >> 3, d0 = I & 7;
  asm volatile("ds_read_b128 a[%c1:%c2], %0 offset:%c3" :: "v"(c.vkb[d0 & 3] + slot), "i"(AK_(kb, d0)), "i"(AK_(kb, d0) + 3), "i"(8192 * kb + 128 * (d0 >> 2)) : "memory", W4_ACLOB_HI);
}
template <int I> DI void q_write1(const u32x4& w) {
  constexpr int aq = AQ_(I >> 3, I & 7);
  asm volatile("v_accvgpr_write_b32 a[%c4], %0\n\tv_accvgpr_write_b32 a[%c5], %1\n\tv_accvgpr_write_b32 a[%c6], %2\n\tv_accvgpr_write_b32 a[%c7], %3"
               :: "v"(w[0]), "v"(w[1]), "v"(w[2]), "v"(w[3]), "i"(aq), "i"(aq + 1), "i"(aq + 2), "i"(aq + 3) : W4_ACLOB_HI);
}
DI void q_write(const int qb, const int d0, const u32x4& w) {
  switch (8 * qb + d0) {
    case 0: q_write1<0>(w); break;   case 1: q_write1<1>(w); break;   case 2: q_write1<2>(w); break;   case 3: q_write1<3>(w); break;
    case 4: q_write1<4>(w); break;   case 5: q_write1<5>(w); break;   case 6: q_write1<6>(w); break;   case 7: q_write1<7>(w); break;
    case 8: q_write1<8>(w); break;   case 9: q_write1<9>(w); break;   case 10: q_write1<10>(w); break; case 11: q_write1<11>(w); break;
    case 12: q_write1<12>(w); break; case 13: q_write1<13>(w); break; case 14: q_write1<14>(w); break; case 15: q_write1<15>(w); break;
  }
}
#define MAINLOOP_ASM \
  "s_add_u32 m0, %[m0w], 32768\n\t" \
  "s_add_u32 s44, %[sofw], 0\n\t" \
  "buffer_load_dwordx4 v130, %[srdw], s44 offen lds\n\t" \
  "s_add_u32 m0, %[m0w], 33792\n\t" \
  "s_add_u32 s44, %[sofw], 128\n\t" \
  "buffer_load_dwordx4 v130, %[srdw], s44 offen lds\n\t" \
  "s_add_u32 m0, %[m0w], 34816\n\t" \
  "s_add_u32 s44, %[sofw], 65536\n\t" \
  "buffer_load_dwordx4 v130, %[srdw], s44 offen lds\n\t" \
  "s_add_u32 m0, %[m0w], 35840\n\t" \
  "s_add_u32 s44, %[sofw], 65664\n\t" \
  "buffer_load_dwordx4 v130, %[srdw], s44 offen lds\n\t" \
  "s_mov_b32 s45, 0\n\t" \
  "buffer_load_dwordx4 a[0:3], v131, %[srdx], s45 offen offset:0\n\t" \
  "s_mov_b32 s45, 8192\n\t" \
  "buffer_load_dwordx4 a[4:7], v131, %[srdx], s45 offen offset:0\n\t" \
  "s_mov_b32 s45, 16384\n\t" \
  "buffer_load_dwordx4 a[8:11], v131, %[srdx], s45 offen offset:0\n\t" \
  "s_mov_b32 s45, 24576\n\t" \
  "buffer_load_dwordx4 a[12:15], v131, %[srdx], s45 offen offset:0\n\t" \
  "s_mov_b32 s45, 32768\n\t" \
  "buffer_load_dwordx4 a[16:19], v131, %[srdx], s45 offen offset:0\n\t" \
  "s_mov_b32 s45, 40960\n\t" \
  "buffer_load_dwordx4 a[20:23], v131, %[srdx], s45 offen offset:0\n\t" \
  "s_mov_b32 s45, 49152\n\t" \
  "buffer_load_dwordx4 a[24:27], v131, %[srdx], s45 offen offset:0\n\t" \
  "s_mov_b32 s45, 57344\n\t" \
  "buffer_load_dwordx4 a[28:31], v131, %[srdx], s45 offen offset:0\n\t" \
  "s_add_u32 m0, %[m0w], 98304\n\t" \
  "s_add_u32 s44, %[sofw], 524288\n\t" \
  "buffer_load_dwordx4 v130, %[srdw], s44 offen lds\n\t" \
  "s_add_u32 m0, %[m0w], 99328\n\t" \
  "s_add_u32 s44, %[sofw], 524416\n\t" \
  "buffer_load_dwordx4 v130, %[srdw], s44 offen lds\n\t" \
  "s_add_u32 m0, %[m0w], 100352\n\t" \
  "s_add_u32 s44, %[sofw], 589824\n\t" \
  "buffer_load_dwordx4 v130, %[srdw], s44 offen lds\n\t" \
  "s_add_u32 m0, %[m0w], 101376\n\t" \
  "s_add_u32 s44, %[sofw], 589952\n\t" \
  "buffer_load_dwordx4 v130, %[srdw], s44 offen lds\n\t" \
  "s_mov_b32 s45, 0\n\t" \
  "buffer_load_dwordx4 a[32:35], v131, %[srdx], s45 offen offset:128\n\t" \
  "s_mov_b32 s45, 8192\n\t" \
  "buffer_load_dwordx4 a[36:39], v131, %[srdx], s45 offen offset:128\n\t" \
  "s_mov_b32 s45, 16384\n\t" \
  "buffer_load_dwordx4 a[40:43], v131, %[srdx], s45 offen offset:128\n\t" \
  "s_mov_b32 s45, 24576\n\t" \
  "buffer_load_dwordx4 a[44:47], v131, %[srdx], s45 offen offset:128\n\t" \
  "s_mov_b32 s45, 32768\n\t" \
  "buffer_load_dwordx4 a[48:51], v131, %[srdx], s45 offen offset:128\n\t" \
  "s_mov_b32 s45, 40960\n\t" \
  "buffer_load_dwordx4 a[52:55], v131, %[srdx], s45 offen offset:128\n\t" \
  "s_mov_b32 s45, 49152\n\t" \
  "buffer_load_dwordx4 a[56:59], v131, %[srdx], s45 offen offset:128\n\t" \
  "s_mov_b32 s45, 57344\n\t" \
  "buffer_load_dwordx4 a[60:63], v131, %[srdx], s45 offen offset:128\n\t" \
  "s_add_u32 m0, %[m0w], 49152\n\t" \
  "s_add_u32 s44, %[sofw], 1048576\n\t" \
  "buffer_load_dwordx4 v130, %[srdw], s44 offen lds\n\t" \
  "s_add_u32 m0, %[m0w], 50176\n\t" \
  "s_add_u32 s44, %[sofw], 1048704\n\t" \
  "buffer_load_dwordx4 v130, %[srdw], s44 offen lds\n\t" \
  "s_add_u32 m0, %[m0w], 51200\n\t" \
  "s_add_u32 s44, %[sofw], 1114112\n\t" \
  "buffer_load_dwordx4 v130, %[srdw], s44 offen lds\n\t" \
  "s_add_u32 m0, %[m0w], 52224\n\t" \
  "s_add_u32 s44, %[sofw], 1114240\n\t" \
  "buffer_load_dwordx4 v130, %[srdw], s44 offen lds\n\t" \
  "s_mov_b32 s45, 0\n\t" \
  "buffer_load_dwordx4 a[64:67], v131, %[srdx], s45 offen offset:256\n\t" \
  "s_mov_b32 s45, 8192\n\t" \
  "buffer_load_dwordx4 a[68:71], v131, %[srdx], s45 offen offset:256\n\t" \
  "s_mov_b32 s45, 16384\n\t" \
  "buffer_load_dwordx4 a[72:75], v131, %[srdx], s45 offen offset:256\n\t" \
  "s_mov_b32 s45, 24576\n\t" \
  "buffer_load_dwordx4 a[76:79], v131, %[srdx], s45 offen offset:256\n\t" \
  "s_mov_b32 s45, 32768\n\t" \
  "buffer_load_dwordx4 a[80:83], v131, %[srdx], s45 offen offset:256\n\t" \
  "s_mov_b32 s45, 40960\n\t" \
  "buffer_load_dwordx4 a[84:87], v131, %[srdx], s45 offen offset:256\n\t" \
  "s_mov_b32 s45, 49152\n\t" \
  "buffer_load_dwordx4 a[88:91], v131, %[srdx], s45 offen offset:256\n\t" \
  "s_mov_b32 s45, 57344\n\t" \
  "buffer_load_dwordx4 a[92:95], v131, %[srdx], s45 offen offset:256\n\t" \
  "s_getpc_b64 s[44:45]\n\t" \
  "s_add_u32 s44, s44, attn_fused@rel32@lo+4\n\t" \
  "s_addc_u32 s45, s45, attn_fused@rel32@hi+12\n\t" \
  "s_mov_b32 s46, k2_code_end-attn_fused\n\t" \
  "s_mov_b32 s47, 0x20000\n\t" \
  "s_mov_b32 m0, %[pfm0]\n\t" \
  "s_nop 0\n\t" \
  "buffer_load_dwordx4 v133, s[44:47], %[pfoff] offen lds\n\t" \
  "s_waitcnt vmcnt(25)\n\t" \
  "s_barrier\n\t" \
  "ds_write_b128 v136, a[0:3] offset:0\n\t" \
  "ds_write_b128 v137, a[4:7] offset:1024\n\t" \
  "ds_write_b128 v136, a[8:11] offset:2048\n\t" \
  "ds_write_b128 v137, a[12:15] offset:3072\n\t" \
  "ds_write_b128 v136, a[16:19] offset:4096\n\t" \
  "ds_write_b128 v137, a[20:23] offset:5120\n\t" \
  "ds_write_b128 v136, a[24:27] offset:6144\n\t" \
  "ds_write_b128 v137, a[28:31] offset:7168\n\t" \
  "ds_read_b128 a[128:131], v138 offset:0\n\t" \
  "ds_read_b128 a[144:147], v138 offset:4096\n\t" \
  "ds_read_b128 a[132:135], v139 offset:0\n\t" \
  "ds_read_b128 a[148:151], v139 offset:4096\n\t" \
  "ds_read_b128 a[136:139], v140 offset:0\n\t" \
  "ds_read_b128 a[152:155], v140 offset:4096\n\t" \
  "ds_read_b128 a[140:143], v141 offset:0\n\t" \
  "ds_read_b128 a[156:159], v141 offset:4096\n\t" \
  "ds_read_b64_tr_b16 a[192:193], v128 offset:32768\n\t" \
  "ds_read_b64_tr_b16 a[194:195], v128 offset:33024\n\t" \
  "ds_read_b64_tr_b16 a[196:197], v128 offset:33280\n\t" \
  "ds_read_b64_tr_b16 a[198:199], v128 offset:33536\n\t" \
  "ds_read_b64_tr_b16 a[200:201], v128 offset:33792\n\t" \
  "ds_read_b64_tr_b16 a[202:203], v128 offset:34048\n\t" \
  "ds_read_b64_tr_b16 a[204:205], v128 offset:34304\n\t" \
  "ds_read_b64_tr_b16 a[206:207], v128 offset:34560\n\t" \
  "s_waitcnt vmcnt(13)\n\t" \
  "ds_write_b128 v136, a[32:35] offset:0\n\t" \
  "s_waitcnt lgkmcnt(7)\n\t" \
  "v_mfma_f32_32x32x16_bf16 v[0:15], a[192:195], a[128:131], 0\n\t" \
  "ds_read_b64_tr_b16 a[208:209], v128 offset:36864\n\t" \
  "v_mfma_f32_32x32x16_bf16 v[16:31], a[192:195], a[144:147], 0\n\t" \
  "ds_read_b64_tr_b16 a[210:211], v128 offset:37120\n\t" \
  "ds_write_b128 v137, a[36:39] offset:1024\n\t" \
  "s_waitcnt lgkmcnt(8)\n\t" \
  "v_mfma_f32_32x32x16_bf16 v[32:47], a[196:199], a[128:131], 0\n\t" \
  "s_waitcnt vmcnt(21)\n\t" \
  "s_barrier\n\t" \
  "ds_read_b64_tr_b16 a[212:213], v128 offset:37376\n\t" \
  "s_mov_b32 s45, 0\n\t" \
  "v_mfma_f32_32x32x16_bf16 v[48:63], a[196:199], a[144:147], 0\n\t" \
  "ds_read_b64_tr_b16 a[214:215], v128 offset:37632\n\t" \
  "buffer_load_dwordx4 a[96:99], v131, %[srdx], s45 offen offset:384\n\t" \
  "s_add_u32 m0, %[m0w], 114688\n\t" \
  "s_add_u32 s44, %[sofw], 1572864\n\t" \
  "s_waitcnt lgkmcnt(8)\n\t" \
  "v_mfma_f32_32x32x16_bf16 v[64:79], a[200:203], a[128:131], 0\n\t" \
  "ds_read_b64_tr_b16 a[216:217], v128 offset:37888\n\t" \
  "buffer_load_dwordx4 v130, %[srdw], s44 offen lds\n\t" \
  "v_mfma_f32_32x32x16_bf16 v[80:95], a[200:203], a[144:147], 0\n\t" \
  "ds_read_b64_tr_b16 a[218:219], v128 offset:38144\n\t" \
  "ds_write_b128 v136, a[40:43] offset:2048\n\t" \
  "s_waitcnt lgkmcnt(9)\n\t" \
  "v_mfma_f32_32x32x16_bf16 v[96:111], a[204:207], a[128:131], 0\n\t" \
  "ds_read_b64_tr_b16 a[220:221], v128 offset:38400\n\t" \
  "s_mov_b32 s45, 8192\n\t" \
  "v_mfma_f32_32x32x16_bf16 v[112:127], a[204:207], a[144:147], 0\n\t" \
  "ds_read_b64_tr_b16 a[222:223], v128 offset:38656\n\t" \
  "buffer_load_dwordx4 a[100:103], v131, %[srdx], s45 offen offset:384\n\t" \
  "s_add_u32 m0, %[m0w], 115712\n\t" \
  "s_add_u32 s44, %[sofw], 1572992\n\t" \
  "s_waitcnt lgkmcnt(8)\n\t" \
  "v_mfma_f32_32x32x16_bf16 v[0:15], a[208:211], a[132:135], v[0:15]\n\t" \
  "ds_read_b64_tr_b16 a[224:225], v128 offset:40960\n\t" \
  "buffer_load_dwordx4 v130, %[srdw], s44 offen lds\n\t" \
  "v_mfma_f32_32x32x16_bf16 v[16:31], a[208:211], a[148:151], v[16:31]\n\t" \
  "ds_read_b64_tr_b16 a[226:227], v128 offset:41216\n\t" \
  "ds_write_b128 v137, a[44:47] offset:3072\n\t" \
  "s_waitcnt lgkmcnt(8)\n\t" \
  "v_mfma_f32_32x32x16_bf16 v[32:47], a[212:215], a[132:135], v[32:47]\n\t" \
  "ds_read_b64_tr_b16 a[228:229], v128 offset:41472\n\t" \
  "s_mov_b32 s45, 16384\n\t" \
  "v_mfma_f32_32x32x16_bf16 v[48:63], a[212:215], a[148:151], v[48:63]\n\t" \
  "ds_read_b64_tr_b16 a[230:231], v128 offset:41728\n\t" \
  "ds_read_b128 a[160:163], v138 offset:0\n\t" \
  "buffer_load_dwordx4 a[104:107], v131, %[srdx], s45 offen offset:384\n\t" \
  "s_add_u32 m0, %[m0w], 116736\n\t" \
  "s_add_u32 s44, %[sofw], 1638400\n\t" \
  "s_waitcnt lgkmcnt(9)\n\t" \
  "v_mfma_f32_32x32x16_bf16 v[64:79], a[216:219], a[132:135], v[64:79]\n\t" \
  "ds_read_b64_tr_b16 a[232:233], v128 offset:41984\n\t" \
  "ds_read_b128 a[164:167], v139 offset:0\n\t" \
  "buffer_load_dwordx4 v130, %[srdw], s44 offen lds\n\t" \
  "v_mfma_f32_32x32x16_bf16 v[80:95], a[216:219], a[148:151], v[80:95]\n\t" \
  "ds_read_b64_tr_b16 a[234:235], v128 offset:42240\n\t" \
  "ds_write_b128 v136, a[48:51] offset:4096\n\t" \
  "ds_read_b128 a[168:171], v140 offset:0\n\t" \
  "s_waitcnt lgkmcnt(11)\n\t" \
  "v_mfma_f32_32x32x16_bf16 v[96:111], a[220:223], a[132:135], v[96:111]\n\t" \
  "ds_read_b64_tr_b16 a[236:237], v128 offset:42496\n\t" \
  "ds_read_b128 a[172:175], v141 offset:0\n\t" \
  "s_mov_b32 s45, 24576\n\t" \
  "v_mfma_f32_32x32x16_bf16 v[112:127], a[220:223], a[148:151], v[112:127]\n\t" \
  "ds_read_b64_tr_b16 a[238:239], v128 offset:42752\n\t" \
  "buffer_load_dwordx4 a[108:111], v131, %[srdx], s45 offen offset:384\n\t" \
  "s_add_u32 m0, %[m0w], 117760\n\t" \
  "s_add_u32 s44, %[sofw], 1638528\n\t" \
  "s_waitcnt lgkmcnt(12)\n\t" \
  "v_mfma_f32_32x32x16_bf16 v[0:15], a[224:227], a[136:139], v[0:15]\n\t" \
  "ds_read_b64_tr_b16 a[240:241], v128 offset:45056\n\t" \
  "buffer_load_dwordx4 v130, %[srdw], s44 offen lds\n\t" \
  "v_mfma_f32_32x32x16_bf16 v[16:31], a[224:227], a[152:155], v[16:31]\n\t" \
  "ds_read_b64_tr_b16 a[242:243], v128 offset:45312\n\t" \
  "ds_write_b128 v137, a[52:55] offset:5120\n\t" \
  "s_waitcnt lgkmcnt(12)\n\t" \
  "v_mfma_f32_32x32x16_bf16 v[32:47], a[228:231], a[136:139], v[32:47]\n\t" \
  "ds_read_b64_tr_b16 a[244:245], v128 offset:45568\n\t" \
  "s_mov_b32 s45, 32768\n\t" \
  "v_mfma_f32_32x32x16_bf16 v[48:63], a[228:231], a[152:155], v[48:63]\n\t" \
  "ds_read_b64_tr_b16 a[246:247], v128 offset:45824\n\t" \
  "buffer_load_dwordx4 a[112:115], v131, %[srdx], s45 offen offset:384\n\t" \
  "s_add_u32 m0, %[m0k], 0\n\t" \
  "s_add_u32 s44, %[sofk], 0\n\t" \
  "s_waitcnt lgkmcnt(10)\n\t" \
  "v_mfma_f32_32x32x16_bf16 v[64:79], a[232:235], a[136:139], v[64:79]\n\t" \
  "ds_read_b64_tr_b16 a[248:249], v128 offset:46080\n\t" \
  "buffer_load_dwordx4 v229, %[srdk], s44 offen nt lds\n\t" \
  "v_mfma_f32_32x32x16_bf16 v[80:95], a[232:235], a[152:155], v[80:95]\n\t" \
  "ds_read_b64_tr_b16 a[250:251], v128 offset:46336\n\t" \
  "ds_write_b128 v136, a[56:59] offset:6144\n\t" \
  "s_waitcnt lgkmcnt(8)\n\t" \
  "v_mfma_f32_32x32x16_bf16 v[96:111], a[236:239], a[136:139], v[96:111]\n\t" \
  "ds_read_b64_tr_b16 a[252:253], v128 offset:46592\n\t" \
  "s_mov_b32 s45, 40960\n\t" \
  "v_mfma_f32_32x32x16_bf16 v[112:127], a[236:239], a[152:155], v[112:127]\n\t" \
  "ds_read_b64_tr_b16 a[254:255], v128 offset:46848\n\t" \
  "buffer_load_dwordx4 a[116:119], v131, %[srdx], s45 offen offset:384\n\t" \
  "s_waitcnt lgkmcnt(8)\n\t" \
  "v_mfma_f32_32x32x16_bf16 v[0:15], a[240:243], a[140:143], v[0:15]\n\t" \
  "ds_read_b64_tr_b16 a[192:193], v129 offset:32768\n\t" \
  "buffer_load_dwordx4 v230, %[srdk], s44 offen offset:1024 nt lds\n\t" \
  "v_mfma_f32_32x32x16_bf16 v[16:31], a[240:243], a[156:159], v[16:31]\n\t" \
  "ds_read_b64_tr_b16 a[194:195], v129 offset:33024\n\t" \
  "ds_write_b128 v137, a[60:63] offset:7168\n\t" \
  "s_waitcnt lgkmcnt(8)\n\t" \
  "v_mfma_f32_32x32x16_bf16 v[32:47], a[244:247], a[140:143], v[32:47]\n\t" \
  "ds_read_b64_tr_b16 a[196:197], v129 offset:33280\n\t" \
  "s_mov_b32 s45, 49152\n\t" \
  "v_mfma_f32_32x32x16_bf16 v[48:63], a[244:247], a[156:159], v[48:63]\n\t" \
  "ds_read_b64_tr_b16 a[198:199], v129 offset:33536\n\t" \
  "ds_read_b128 a[176:179], v138 offset:4096\n\t" \
  "buffer_load_dwordx4 a[120:123], v131, %[srdx], s45 offen offset:384\n\t" \
  "s_waitcnt lgkmcnt(9)\n\t" \
  "v_mfma_f32_32x32x16_bf16 v[64:79], a[248:251], a[140:143], v[64:79]\n\t" \
  "s_waitcnt vmcnt(14)\n\t" \
  "ds_read_b64_tr_b16 a[200:201], v129 offset:33792\n\t" \
  "ds_read_b128 a[180:183], v139 offset:4096\n\t" \
  "buffer_load_dwordx4 v229, %[srdk], s44 offen offset:2048 nt lds\n\t" \
  "v_mfma_f32_32x32x16_bf16 v[80:95], a[248:251], a[156:159], v[80:95]\n\t" \
  "ds_read_b64_tr_b16 a[202:203], v129 offset:34048\n\t" \
  "ds_read_b128 a[184:187], v140 offset:4096\n\t" \
  "ds_write_b128 v136, a[64:67] offset:0\n\t" \
  "s_waitcnt lgkmcnt(11)\n\t" \
  "v_mfma_f32_32x32x16_bf16 v[96:111], a[252:255], a[140:143], v[96:111]\n\t" \
  "ds_read_b64_tr_b16 a[204:205], v129 offset:34304\n\t" \
  "ds_read_b128 a[188:191], v141 offset:4096\n\t" \
  "buffer_load_dwordx4 v230, %[srdk], s44 offen offset:3072 nt lds\n\t" \
  "s_mov_b32 s45, 57344\n\t" \
  "v_mfma_f32_32x32x16_bf16 v[112:127], a[252:255], a[156:159], v[112:127]\n\t" \
  "ds_read_b64_tr_b16 a[206:207], v129 offset:34560\n\t" \
  "buffer_load_dwordx4 a[124:127], v131, %[srdx], s45 offen offset:384\n\t" \
  "s_waitcnt lgkmcnt(12)\n\t" \
  "v_mfma_f32_32x32x16_bf16 v[0:15], a[192:195], a[160:163], v[0:15]\n\t" \
  "ds_read_b64_tr_b16 a[208:209], v129 offset:36864\n\t" \
  "s_waitcnt lgkmcnt(9)\n\t" \
  "v_mfma_f32_32x32x16_bf16 v[16:31], a[192:195], a[176:179], v[16:31]\n\t" \
  "ds_read_b64_tr_b16 a[210:211], v129 offset:37120\n\t" \
  "ds_write_b128 v137, a[68:71] offset:1024\n\t" \
  "v_mfma_f32_32x32x16_bf16 v[32:47], a[196:199], a[160:163], v[32:47]\n\t" \
  "s_waitcnt vmcnt(25)\n\t" \
  "s_barrier\n\t" \
  "ds_read_b64_tr_b16 a[212:213], v129 offset:37376\n\t" \
  "s_mov_b32 s45, 0\n\t" \
  "v_mfma_f32_32x32x16_bf16 v[48:63], a[196:199], a[176:179], v[48:63]\n\t" \
  "ds_read_b64_tr_b16 a[214:215], v129 offset:37632\n\t" \
  "buffer_load_dwordx4 a[0:3], v131, %[srdx], s45 offen offset:512\n\t" \
  "s_add_u32 m0, %[m0w], 32768\n\t" \
  "s_add_u32 s44, %[sofw], 2097152\n\t" \
  "s_waitcnt lgkmcnt(10)\n\t" \
  "v_mfma_f32_32x32x16_bf16 v[64:79], a[200:203], a[160:163], v[64:79]\n\t" \
  "ds_read_b64_tr_b16 a[216:217], v129 offset:37888\n\t" \
  "buffer_load_dwordx4 v130, %[srdw], s44 offen lds\n\t" \
  "v_mfma_f32_32x32x16_bf16 v[80:95], a[200:203], a[176:179], v[80:95]\n\t" \
  "ds_read_b64_tr_b16 a[218:219], v129 offset:38144\n\t" \
  "ds_write_b128 v136, a[72:75] offset:2048\n\t" \
  "s_waitcnt lgkmcnt(8)\n\t" \
  "v_mfma_f32_32x32x16_bf16 v[96:111], a[204:207], a[160:163], v[96:111]\n\t" \
  "ds_read_b64_tr_b16 a[220:221], v129 offset:38400\n\t" \
  "s_mov_b32 s45, 8192\n\t" \
  "v_mfma_f32_32x32x16_bf16 v[112:127], a[204:207], a[176:179], v[112:127]\n\t" \
  "ds_read_b64_tr_b16 a[222:223], v129 offset:38656\n\t" \
  "buffer_load_dwordx4 a[4:7], v131, %[srdx], s45 offen offset:512\n\t" \
  "s_add_u32 m0, %[m0w], 33792\n\t" \
  "s_add_u32 s44, %[sofw], 2097280\n\t" \
  "s_waitcnt lgkmcnt(8)\n\t" \
  "v_mfma_f32_32x32x16_bf16 v[0:15], a[208:211], a[164:167], v[0:15]\n\t" \
  "ds_read_b64_tr_b16 a[224:225], v129 offset:40960\n\t" \
  "buffer_load_dwordx4 v130, %[srdw], s44 offen lds\n\t" \
  "v_mfma_f32_32x32x16_bf16 v[16:31], a[208:211], a[180:183], v[16:31]\n\t" \
  "ds_read_b64_tr_b16 a[226:227], v129 offset:41216\n\t" \
  "ds_write_b128 v137, a[76:79] offset:3072\n\t" \
  "s_waitcnt lgkmcnt(8)\n\t" \
  "v_mfma_f32_32x32x16_bf16 v[32:47], a[212:215], a[164:167], v[32:47]\n\t" \
  "ds_read_b64_tr_b16 a[228:229], v129 offset:41472\n\t" \
  "s_mov_b32 s45, 16384\n\t" \
  "v_mfma_f32_32x32x16_bf16 v[48:63], a[212:215], a[180:183], v[48:63]\n\t" \
  "ds_read_b64_tr_b16 a[230:231], v129 offset:41728\n\t" \
  "ds_read_b128 a[128:131], v138 offset:0\n\t" \
  "buffer_load_dwordx4 a[8:11], v131, %[srdx], s45 offen offset:512\n\t" \
  "s_add_u32 m0, %[m0w], 34816\n\t" \
  "s_add_u32 s44, %[sofw], 2162688\n\t" \
  "s_waitcnt lgkmcnt(9)\n\t" \
  "v_mfma_f32_32x32x16_bf16 v[64:79], a[216:219], a[164:167], v[64:79]\n\t" \
  "ds_read_b64_tr_b16 a[232:233], v129 offset:41984\n\t" \
  "ds_read_b128 a[132:135], v139 offset:0\n\t" \
  "buffer_load_dwordx4 v130, %[srdw], s44 offen lds\n\t" \
  "v_mfma_f32_32x32x16_bf16 v[80:95], a[216:219], a[180:183], v[80:95]\n\t" \
  "ds_read_b64_tr_b16 a[234:235], v129 offset:42240\n\t" \
  "ds_write_b128 v136, a[80:83] offset:4096\n\t" \
  "ds_read_b128 a[136:139], v140 offset:0\n\t" \
  "s_waitcnt lgkmcnt(11)\n\t" \
  "v_mfma_f32_32x32x16_bf16 v[96:111], a[220:223], a[164:167], v[96:111]\n\t" \
  "ds_read_b64_tr_b16 a[236:237], v129 offset:42496\n\t" \
  "ds_read_b128 a[140:143], v141 offset:0\n\t" \
  "s_mov_b32 s45, 24576\n\t" \
  "v_mfma_f32_32x32x16_bf16 v[112:127], a[220:223], a[180:183], v[112:127]\n\t" \
  "ds_read_b64_tr_b16 a[238:239], v129 offset:42752\n\t" \
  "buffer_load_dwordx4 a[12:15], v131, %[srdx], s45 offen offset:512\n\t" \
  "s_add_u32 m0, %[m0w], 35840\n\t" \
  "s_add_u32 s44, %[sofw], 2162816\n\t" \
  "s_waitcnt lgkmcnt(12)\n\t" \
  "v_mfma_f32_32x32x16_bf16 v[0:15], a[224:227], a[168:171], v[0:15]\n\t" \
  "ds_read_b64_tr_b16 a[240:241], v129 offset:45056\n\t" \
  "buffer_load_dwordx4 v130, %[srdw], s44 offen lds\n\t" \
  "v_mfma_f32_32x32x16_bf16 v[16:31], a[224:227], a[184:187], v[16:31]\n\t" \
  "ds_read_b64_tr_b16 a[242:243], v129 offset:45312\n\t" \
  "ds_write_b128 v137, a[84:87] offset:5120\n\t" \
  "s_waitcnt lgkmcnt(12)\n\t" \
  "v_mfma_f32_32x32x16_bf16 v[32:47], a[228:231], a[168:171], v[32:47]\n\t" \
  "ds_read_b64_tr_b16 a[244:245], v129 offset:45568\n\t" \
  "s_mov_b32 s45, 32768\n\t" \
  "v_mfma_f32_32x32x16_bf16 v[48:63], a[228:231], a[184:187], v[48:63]\n\t" \
  "ds_read_b64_tr_b16 a[246:247], v129 offset:45824\n\t" \
  "buffer_load_dwordx4 a[16:19], v131, %[srdx], s45 offen offset:512\n\t" \
  "s_add_u32 m0, %[m0v], 0\n\t" \
  "s_add_u32 s44, %[sofv], 0\n\t" \
  "s_waitcnt lgkmcnt(10)\n\t" \
  "v_mfma_f32_32x32x16_bf16 v[64:79], a[232:235], a[168:171], v[64:79]\n\t" \
  "ds_read_b64_tr_b16 a[248:249], v129 offset:46080\n\t" \
  "buffer_load_dwordx4 v231, %[srdv], s44 offen nt lds\n\t" \
  "v_mfma_f32_32x32x16_bf16 v[80:95], a[232:235], a[184:187], v[80:95]\n\t" \
  "ds_read_b64_tr_b16 a[250:251], v129 offset:46336\n\t" \
  "ds_write_b128 v136, a[88:91] offset:6144\n\t" \
  "s_waitcnt lgkmcnt(8)\n\t" \
  "v_mfma_f32_32x32x16_bf16 v[96:111], a[236:239], a[168:171], v[96:111]\n\t" \
  "ds_read_b64_tr_b16 a[252:253], v129 offset:46592\n\t" \
  "s_mov_b32 s45, 40960\n\t" \
  "v_mfma_f32_32x32x16_bf16 v[112:127], a[236:239], a[184:187], v[112:127]\n\t" \
  "ds_read_b64_tr_b16 a[254:255], v129 offset:46848\n\t" \
  "buffer_load_dwordx4 a[20:23], v131, %[srdx], s45 offen offset:512\n\t" \
  "s_waitcnt lgkmcnt(8)\n\t" \
  "v_mfma_f32_32x32x16_bf16 v[0:15], a[240:243], a[172:175], v[0:15]\n\t" \
  "ds_read_b64_tr_b16 a[192:193], v128 offset:49152\n\t" \
  "buffer_load_dwordx4 v231, %[srdv], s44 offen offset:2048 nt lds\n\t" \
  "v_mfma_f32_32x32x16_bf16 v[16:31], a[240:243], a[188:191], v[16:31]\n\t" \
  "ds_read_b64_tr_b16 a[194:195], v128 offset:49408\n\t" \
  "ds_write_b128 v137, a[92:95] offset:7168\n\t" \
  "s_waitcnt lgkmcnt(8)\n\t" \
  "v_mfma_f32_32x32x16_bf16 v[32:47], a[244:247], a[172:175], v[32:47]\n\t" \
  "ds_read_b64_tr_b16 a[196:197], v128 offset:49664\n\t" \
  "s_mov_b32 s45, 49152\n\t" \
  "v_mfma_f32_32x32x16_bf16 v[48:63], a[244:247], a[188:191], v[48:63]\n\t" \
  "ds_read_b64_tr_b16 a[198:199], v128 offset:49920\n\t" \
  "ds_read_b128 a[144:147], v138 offset:4096\n\t" \
  "buffer_load_dwordx4 a[24:27], v131, %[srdx], s45 offen offset:512\n\t" \
  "s_add_u32 m0, %[m0v], 4096\n\t" \
  "s_add_u32 s44, %[sofv], 0\n\t" \
  "s_waitcnt lgkmcnt(9)\n\t" \
  "v_mfma_f32_32x32x16_bf16 v[64:79], a[248:251], a[172:175], v[64:79]\n\t" \
  "s_waitcnt vmcnt(13)\n\t" \
  "ds_read_b64_tr_b16 a[200:201], v128 offset:50176\n\t" \
  "ds_read_b128 a[148:151], v139 offset:4096\n\t" \
  "buffer_load_dwordx4 v232, %[srdv], s44 offen nt lds\n\t" \
  "v_mfma_f32_32x32x16_bf16 v[80:95], a[248:251], a[188:191], v[80:95]\n\t" \
  "ds_read_b64_tr_b16 a[202:203], v128 offset:50432\n\t" \
  "ds_read_b128 a[152:155], v140 offset:4096\n\t" \
  "ds_write_b128 v136, a[96:99] offset:0\n\t" \
  "s_waitcnt lgkmcnt(11)\n\t" \
  "v_mfma_f32_32x32x16_bf16 v[96:111], a[252:255], a[172:175], v[96:111]\n\t" \
  "ds_read_b64_tr_b16 a[204:205], v128 offset:50688\n\t" \
  "ds_read_b128 a[156:159], v141 offset:4096\n\t" \
  "buffer_load_dwordx4 v232, %[srdv], s44 offen offset:2048 nt lds\n\t" \
  "s_mov_b32 s45, 57344\n\t" \
  "v_mfma_f32_32x32x16_bf16 v[112:127], a[252:255], a[188:191], v[112:127]\n\t" \
  "ds_read_b64_tr_b16 a[206:207], v128 offset:50944\n\t" \
  "buffer_load_dwordx4 a[28:31], v131, %[srdx], s45 offen offset:512\n\t" \
  "s_waitcnt lgkmcnt(12)\n\t" \
  "v_mfma_f32_32x32x16_bf16 v[0:15], a[192:195], a[128:131], v[0:15]\n\t" \
  "ds_read_b64_tr_b16 a[208:209], v128 offset:53248\n\t" \
  "s_waitcnt lgkmcnt(9)\n\t" \
  "v_mfma_f32_32x32x16_bf16 v[16:31], a[192:195], a[144:147], v[16:31]\n\t" \
  "ds_read_b64_tr_b16 a[210:211], v128 offset:53504\n\t" \
  "ds_write_b128 v137, a[100:103] offset:1024\n\t" \
  "v_mfma_f32_32x32x16_bf16 v[32:47], a[196:199], a[128:131], v[32:47]\n\t" \
  "s_waitcnt vmcnt(24)\n\t" \
  "s_barrier\n\t" \
  "ds_read_b64_tr_b16 a[212:213], v128 offset:53760\n\t" \
  "s_mov_b32 s45, 0\n\t" \
  "v_mfma_f32_32x32x16_bf16 v[48:63], a[196:199], a[144:147], v[48:63]\n\t" \
  "ds_read_b64_tr_b16 a[214:215], v128 offset:54016\n\t" \
  "buffer_load_dwordx4 a[32:35], v131, %[srdx], s45 offen offset:640\n\t" \
  "s_add_u32 m0, %[m0w], 98304\n\t" \
  "s_add_u32 s44, %[sofw], 2621440\n\t" \
  "s_waitcnt lgkmcnt(10)\n\t" \
  "v_mfma_f32_32x32x16_bf16 v[64:79], a[200:203], a[128:131], v[64:79]\n\t" \
  "ds_read_b64_tr_b16 a[216:217], v128 offset:54272\n\t" \
  "buffer_load_dwordx4 v130, %[srdw], s44 offen lds\n\t" \
  "v_mfma_f32_32x32x16_bf16 v[80:95], a[200:203], a[144:147], v[80:95]\n\t" \
  "ds_read_b64_tr_b16 a[218:219], v128 offset:54528\n\t" \
  "ds_write_b128 v136, a[104:107] offset:2048\n\t" \
  "s_waitcnt lgkmcnt(8)\n\t" \
  "v_mfma_f32_32x32x16_bf16 v[96:111], a[204:207], a[128:131], v[96:111]\n\t" \
  "ds_read_b64_tr_b16 a[220:221], v128 offset:54784\n\t" \
  "s_mov_b32 s45, 8192\n\t" \
  "v_mfma_f32_32x32x16_bf16 v[112:127], a[204:207], a[144:147], v[112:127]\n\t" \
  "ds_read_b64_tr_b16 a[222:223], v128 offset:55040\n\t" \
  "buffer_load_dwordx4 a[36:39], v131, %[srdx], s45 offen offset:640\n\t" \
  "s_add_u32 m0, %[m0w], 99328\n\t" \
  "s_add_u32 s44, %[sofw], 2621568\n\t" \
  "s_waitcnt lgkmcnt(8)\n\t" \
  "v_mfma_f32_32x32x16_bf16 v[0:15], a[208:211], a[132:135], v[0:15]\n\t" \
  "ds_read_b64_tr_b16 a[224:225], v128 offset:57344\n\t" \
  "buffer_load_dwordx4 v130, %[srdw], s44 offen lds\n\t" \
  "v_mfma_f32_32x32x16_bf16 v[16:31], a[208:211], a[148:151], v[16:31]\n\t" \
  "ds_read_b64_tr_b16 a[226:227], v128 offset:57600\n\t" \
  "ds_write_b128 v137, a[108:111] offset:3072\n\t" \
  "s_waitcnt lgkmcnt(8)\n\t" \
  "v_mfma_f32_32x32x16_bf16 v[32:47], a[212:215], a[132:135], v[32:47]\n\t" \
  "ds_read_b64_tr_b16 a[228:229], v128 offset:57856\n\t" \
  "s_mov_b32 s45, 16384\n\t" \
  "v_mfma_f32_32x32x16_bf16 v[48:63], a[212:215], a[148:151], v[48:63]\n\t" \
  "ds_read_b64_tr_b16 a[230:231], v128 offset:58112\n\t" \
  "ds_read_b128 a[160:163], v138 offset:0\n\t" \
  "buffer_load_dwordx4 a[40:43], v131, %[srdx], s45 offen offset:640\n\t" \
  "s_add_u32 m0, %[m0w], 100352\n\t" \
  "s_add_u32 s44, %[sofw], 2686976\n\t" \
  "s_waitcnt lgkmcnt(9)\n\t" \
  "v_mfma_f32_32x32x16_bf16 v[64:79], a[216:219], a[132:135], v[64:79]\n\t" \
  "ds_read_b64_tr_b16 a[232:233], v128 offset:58368\n\t" \
  "ds_read_b128 a[164:167], v139 offset:0\n\t" \
  "buffer_load_dwordx4 v130, %[srdw], s44 offen lds\n\t" \
  "v_mfma_f32_32x32x16_bf16 v[80:95], a[216:219], a[148:151], v[80:95]\n\t" \
  "ds_read_b64_tr_b16 a[234:235], v128 offset:58624\n\t" \
  "ds_write_b128 v136, a[112:115] offset:4096\n\t" \
  "ds_read_b128 a[168:171], v140 offset:0\n\t" \
  "s_waitcnt lgkmcnt(11)\n\t" \
  "v_mfma_f32_32x32x16_bf16 v[96:111], a[220:223], a[132:135], v[96:111]\n\t" \
  "ds_read_b64_tr_b16 a[236:237], v128 offset:58880\n\t" \
  "ds_read_b128 a[172:175], v141 offset:0\n\t" \
  "s_mov_b32 s45, 24576\n\t" \
  "v_mfma_f32_32x32x16_bf16 v[112:127], a[220:223], a[148:151], v[112:127]\n\t" \
  "ds_read_b64_tr_b16 a[238:239], v128 offset:59136\n\t" \
  "buffer_load_dwordx4 a[44:47], v131, %[srdx], s45 offen offset:640\n\t" \
  "s_add_u32 m0, %[m0w], 101376\n\t" \
  "s_add_u32 s44, %[sofw], 2687104\n\t" \
  "s_waitcnt lgkmcnt(12)\n\t" \
  "v_mfma_f32_32x32x16_bf16 v[0:15], a[224:227], a[136:139], v[0:15]\n\t" \
  "ds_read_b64_tr_b16 a[240:241], v128 offset:61440\n\t" \
  "buffer_load_dwordx4 v130, %[srdw], s44 offen lds\n\t" \
  "v_mfma_f32_32x32x16_bf16 v[16:31], a[224:227], a[152:155], v[16:31]\n\t" \
  "ds_read_b64_tr_b16 a[242:243], v128 offset:61696\n\t" \
  "ds_write_b128 v137, a[116:119] offset:5120\n\t" \
  "s_waitcnt lgkmcnt(12)\n\t" \
  "v_mfma_f32_32x32x16_bf16 v[32:47], a[228:231], a[136:139], v[32:47]\n\t" \
  "ds_read_b64_tr_b16 a[244:245], v128 offset:61952\n\t" \
  "s_mov_b32 s45, 32768\n\t" \
  "v_mfma_f32_32x32x16_bf16 v[48:63], a[228:231], a[152:155], v[48:63]\n\t" \
  "ds_read_b64_tr_b16 a[246:247], v128 offset:62208\n\t" \
  "buffer_load_dwordx4 a[48:51], v131, %[srdx], s45 offen offset:640\n\t" \
  "s_add_u32 m0, %[m0k], 16384\n\t" \
  "s_add_u32 s44, %[sofk], 16384\n\t" \
  "s_waitcnt lgkmcnt(10)\n\t" \
  "v_mfma_f32_32x32x16_bf16 v[64:79], a[232:235], a[136:139], v[64:79]\n\t" \
  "ds_read_b64_tr_b16 a[248:249], v128 offset:62464\n\t" \
  "buffer_load_dwordx4 v229, %[srdk], s44 offen nt lds\n\t" \
  "v_mfma_f32_32x32x16_bf16 v[80:95], a[232:235], a[152:155], v[80:95]\n\t" \
  "ds_read_b64_tr_b16 a[250:251], v128 offset:62720\n\t" \
  "ds_write_b128 v136, a[120:123] offset:6144\n\t" \
  "s_waitcnt lgkmcnt(8)\n\t" \
  "v_mfma_f32_32x32x16_bf16 v[96:111], a[236:239], a[136:139], v[96:111]\n\t" \
  "ds_read_b64_tr_b16 a[252:253], v128 offset:62976\n\t" \
  "s_mov_b32 s45, 40960\n\t" \
  "v_mfma_f32_32x32x16_bf16 v[112:127], a[236:239], a[152:155], v[112:127]\n\t" \
  "ds_read_b64_tr_b16 a[254:255], v128 offset:63232\n\t" \
  "buffer_load_dwordx4 a[52:55], v131, %[srdx], s45 offen offset:640\n\t" \
  "s_waitcnt lgkmcnt(8)\n\t" \
  "v_mfma_f32_32x32x16_bf16 v[0:15], a[240:243], a[140:143], v[0:15]\n\t" \
  "ds_read_b64_tr_b16 a[192:193], v129 offset:49152\n\t" \
  "buffer_load_dwordx4 v230, %[srdk], s44 offen offset:1024 nt lds\n\t" \
  "v_mfma_f32_32x32x16_bf16 v[16:31], a[240:243], a[156:159], v[16:31]\n\t" \
  "ds_read_b64_tr_b16 a[194:195], v129 offset:49408\n\t" \
  "ds_write_b128 v137, a[124:127] offset:7168\n\t" \
  "s_waitcnt lgkmcnt(8)\n\t" \
  "v_mfma_f32_32x32x16_bf16 v[32:47], a[244:247], a[140:143], v[32:47]\n\t" \
  "ds_read_b64_tr_b16 a[196:197], v129 offset:49664\n\t" \
  "s_mov_b32 s45, 49152\n\t" \
  "v_mfma_f32_32x32x16_bf16 v[48:63], a[244:247], a[156:159], v[48:63]\n\t" \
  "ds_read_b64_tr_b16 a[198:199], v129 offset:49920\n\t" \
  "ds_read_b128 a[176:179], v138 offset:4096\n\t" \
  "buffer_load_dwordx4 a[56:59], v131, %[srdx], s45 offen offset:640\n\t" \
  "s_waitcnt lgkmcnt(9)\n\t" \
  "v_mfma_f32_32x32x16_bf16 v[64:79], a[248:251], a[140:143], v[64:79]\n\t" \
  "s_waitcnt vmcnt(13)\n\t" \
  "ds_read_b64_tr_b16 a[200:201], v129 offset:50176\n\t" \
  "ds_read_b128 a[180:183], v139 offset:4096\n\t" \
  "buffer_load_dwordx4 v229, %[srdk], s44 offen offset:2048 nt lds\n\t" \
  "v_mfma_f32_32x32x16_bf16 v[80:95], a[248:251], a[156:159], v[80:95]\n\t" \
  "ds_read_b64_tr_b16 a[202:203], v129 offset:50432\n\t" \
  "ds_read_b128 a[184:187], v140 offset:4096\n\t" \
  "ds_write_b128 v136, a[0:3] offset:0\n\t" \
  "s_waitcnt lgkmcnt(11)\n\t" \
  "v_mfma_f32_32x32x16_bf16 v[96:111], a[252:255], a[140:143], v[96:111]\n\t" \
  "ds_read_b64_tr_b16 a[204:205], v129 offset:50688\n\t" \
  "ds_read_b128 a[188:191], v141 offset:4096\n\t" \
  "buffer_load_dwordx4 v230, %[srdk], s44 offen offset:3072 nt lds\n\t" \
  "s_mov_b32 s45, 57344\n\t" \
  "v_mfma_f32_32x32x16_bf16 v[112:127], a[252:255], a[156:159], v[112:127]\n\t" \
  "ds_read_b64_tr_b16 a[206:207], v129 offset:50944\n\t" \
  "buffer_load_dwordx4 a[60:63], v131, %[srdx], s45 offen offset:640\n\t" \
  "s_waitcnt lgkmcnt(12)\n\t" \
  "v_mfma_f32_32x32x16_bf16 v[0:15], a[192:195], a[160:163], v[0:15]\n\t" \
  "ds_read_b64_tr_b16 a[208:209], v129 offset:53248\n\t" \
  "s_waitcnt lgkmcnt(9)\n\t" \
  "v_mfma_f32_32x32x16_bf16 v[16:31], a[192:195], a[176:179], v[16:31]\n\t" \
  "ds_read_b64_tr_b16 a[210:211], v129 offset:53504\n\t" \
  "ds_write_b128 v137, a[4:7] offset:1024\n\t" \
  "v_mfma_f32_32x32x16_bf16 v[32:47], a[196:199], a[160:163], v[32:47]\n\t" \
  "s_waitcnt vmcnt(24)\n\t" \
  "s_barrier\n\t" \
  "ds_read_b64_tr_b16 a[212:213], v129 offset:53760\n\t" \
  "s_mov_b32 s45, 0\n\t" \
  "v_mfma_f32_32x32x16_bf16 v[48:63], a[196:199], a[176:179], v[48:63]\n\t" \
  "ds_read_b64_tr_b16 a[214:215], v129 offset:54016\n\t" \
  "buffer_load_dwordx4 a[64:67], v131, %[srdx], s45 offen offset:768\n\t" \
  "s_add_u32 m0, %[m0w], 49152\n\t" \
  "s_add_u32 s44, %[sofw], 3145728\n\t" \
  "s_waitcnt lgkmcnt(10)\n\t" \
  "v_mfma_f32_32x32x16_bf16 v[64:79], a[200:203], a[160:163], v[64:79]\n\t" \
  "ds_read_b64_tr_b16 a[216:217], v129 offset:54272\n\t" \
  "buffer_load_dwordx4 v130, %[srdw], s44 offen lds\n\t" \
  "v_mfma_f32_32x32x16_bf16 v[80:95], a[200:203], a[176:179], v[80:95]\n\t" \
  "ds_read_b64_tr_b16 a[218:219], v129 offset:54528\n\t" \
  "ds_write_b128 v136, a[8:11] offset:2048\n\t" \
  "s_waitcnt lgkmcnt(8)\n\t" \
  "v_mfma_f32_32x32x16_bf16 v[96:111], a[204:207], a[160:163], v[96:111]\n\t" \
  "ds_read_b64_tr_b16 a[220:221], v129 offset:54784\n\t" \
  "s_mov_b32 s45, 8192\n\t" \
  "v_mfma_f32_32x32x16_bf16 v[112:127], a[204:207], a[176:179], v[112:127]\n\t" \
  "ds_read_b64_tr_b16 a[222:223], v129 offset:55040\n\t" \
  "buffer_load_dwordx4 a[68:71], v131, %[srdx], s45 offen offset:768\n\t" \
  "s_add_u32 m0, %[m0w], 50176\n\t" \
  "s_add_u32 s44, %[sofw], 3145856\n\t" \
  "s_waitcnt lgkmcnt(8)\n\t" \
  "v_mfma_f32_32x32x16_bf16 v[0:15], a[208:211], a[164:167], v[0:15]\n\t" \
  "ds_read_b64_tr_b16 a[224:225], v129 offset:57344\n\t" \
  "buffer_load_dwordx4 v130, %[srdw], s44 offen lds\n\t" \
  "v_mfma_f32_32x32x16_bf16 v[16:31], a[208:211], a[180:183], v[16:31]\n\t" \
  "ds_read_b64_tr_b16 a[226:227], v129 offset:57600\n\t" \
  "ds_write_b128 v137, a[12:15] offset:3072\n\t" \
  "s_waitcnt lgkmcnt(8)\n\t" \
  "v_mfma_f32_32x32x16_bf16 v[32:47], a[212:215], a[164:167], v[32:47]\n\t" \
  "ds_read_b64_tr_b16 a[228:229], v129 offset:57856\n\t" \
  "s_mov_b32 s45, 16384\n\t" \
  "v_mfma_f32_32x32x16_bf16 v[48:63], a[212:215], a[180:183], v[48:63]\n\t" \
  "ds_read_b64_tr_b16 a[230:231], v129 offset:58112\n\t" \
  "ds_read_b128 a[128:131], v138 offset:0\n\t" \
  "buffer_load_dwordx4 a[72:75], v131, %[srdx], s45 offen offset:768\n\t" \
  "s_add_u32 m0, %[m0w], 51200\n\t" \
  "s_add_u32 s44, %[sofw], 3211264\n\t" \
  "s_waitcnt lgkmcnt(9)\n\t" \
  "v_mfma_f32_32x32x16_bf16 v[64:79], a[216:219], a[164:167], v[64:79]\n\t" \
  "ds_read_b64_tr_b16 a[232:233], v129 offset:58368\n\t" \
  "ds_read_b128 a[132:135], v139 offset:0\n\t" \
  "buffer_load_dwordx4 v130, %[srdw], s44 offen lds\n\t" \
  "v_mfma_f32_32x32x16_bf16 v[80:95], a[216:219], a[180:183], v[80:95]\n\t" \
  "ds_read_b64_tr_b16 a[234:235], v129 offset:58624\n\t" \
  "ds_write_b128 v136, a[16:19] offset:4096\n\t" \
  "ds_read_b128 a[136:139], v140 offset:0\n\t" \
  "s_waitcnt lgkmcnt(11)\n\t" \
  "v_mfma_f32_32x32x16_bf16 v[96:111], a[220:223], a[164:167], v[96:111]\n\t" \
  "ds_read_b64_tr_b16 a[236:237], v129 offset:58880\n\t" \
  "ds_read_b128 a[140:143], v141 offset:0\n\t" \
  "s_mov_b32 s45, 24576\n\t" \
  "v_mfma_f32_32x32x16_bf16 v[112:127], a[220:223], a[180:183], v[112:127]\n\t" \
  "ds_read_b64_tr_b16 a[238:239], v129 offset:59136\n\t" \
  "buffer_load_dwordx4 a[76:79], v131, %[srdx], s45 offen offset:768\n\t" \
  "s_add_u32 m0, %[m0w], 52224\n\t" \
  "s_add_u32 s44, %[sofw], 3211392\n\t" \
  "s_waitcnt lgkmcnt(12)\n\t" \
  "v_mfma_f32_32x32x16_bf16 v[0:15], a[224:227], a[168:171], v[0:15]\n\t" \
  "ds_read_b64_tr_b16 a[240:241], v129 offset:61440\n\t" \
  "buffer_load_dwordx4 v130, %[srdw], s44 offen lds\n\t" \
  "v_mfma_f32_32x32x16_bf16 v[16:31], a[224:227], a[184:187], v[16:31]\n\t" \
  "ds_read_b64_tr_b16 a[242:243], v129 offset:61696\n\t" \
  "ds_write_b128 v137, a[20:23] offset:5120\n\t" \
  "s_waitcnt lgkmcnt(12)\n\t" \
  "v_mfma_f32_32x32x16_bf16 v[32:47], a[228:231], a[168:171], v[32:47]\n\t" \
  "ds_read_b64_tr_b16 a[244:245], v129 offset:61952\n\t" \
  "s_mov_b32 s45, 32768\n\t" \
  "v_mfma_f32_32x32x16_bf16 v[48:63], a[228:231], a[184:187], v[48:63]\n\t" \
  "ds_read_b64_tr_b16 a[246:247], v129 offset:62208\n\t" \
  "buffer_load_dwordx4 a[80:83], v131, %[srdx], s45 offen offset:768\n\t" \
  "s_add_u32 m0, %[m0v], 16384\n\t" \
  "s_add_u32 s44, %[sofv], 16384\n\t" \
  "s_waitcnt lgkmcnt(10)\n\t" \
  "v_mfma_f32_32x32x16_bf16 v[64:79], a[232:235], a[168:171], v[64:79]\n\t" \
  "ds_read_b64_tr_b16 a[248:249], v129 offset:62464\n\t" \
  "buffer_load_dwordx4 v231, %[srdv], s44 offen nt lds\n\t" \
  "v_mfma_f32_32x32x16_bf16 v[80:95], a[232:235], a[184:187], v[80:95]\n\t" \
  "ds_read_b64_tr_b16 a[250:251], v129 offset:62720\n\t" \
  "ds_write_b128 v136, a[24:27] offset:6144\n\t" \
  "s_waitcnt lgkmcnt(8)\n\t" \
  "v_mfma_f32_32x32x16_bf16 v[96:111], a[236:239], a[168:171], v[96:111]\n\t" \
  "ds_read_b64_tr_b16 a[252:253], v129 offset:62976\n\t" \
  "s_mov_b32 s45, 40960\n\t" \
  "v_mfma_f32_32x32x16_bf16 v[112:127], a[236:239], a[184:187], v[112:127]\n\t" \
  "ds_read_b64_tr_b16 a[254:255], v129 offset:63232\n\t" \
  "buffer_load_dwordx4 a[84:87], v131, %[srdx], s45 offen offset:768\n\t" \
  "s_waitcnt lgkmcnt(8)\n\t" \
  "v_mfma_f32_32x32x16_bf16 v[0:15], a[240:243], a[172:175], v[0:15]\n\t" \
  "ds_read_b64_tr_b16 a[192:193], v128 offset:32768\n\t" \
  "buffer_load_dwordx4 v231, %[srdv], s44 offen offset:2048 nt lds\n\t" \
  "v_mfma_f32_32x32x16_bf16 v[16:31], a[240:243], a[188:191], v[16:31]\n\t" \
  "ds_read_b64_tr_b16 a[194:195], v128 offset:33024\n\t" \
  "ds_write_b128 v137, a[28:31] offset:7168\n\t" \
  "s_waitcnt lgkmcnt(8)\n\t" \
  "v_mfma_f32_32x32x16_bf16 v[32:47], a[244:247], a[172:175], v[32:47]\n\t" \
  "ds_read_b64_tr_b16 a[196:197], v128 offset:33280\n\t" \
  "s_mov_b32 s45, 49152\n\t" \
  "v_mfma_f32_32x32x16_bf16 v[48:63], a[244:247], a[188:191], v[48:63]\n\t" \
  "ds_read_b64_tr_b16 a[198:199], v128 offset:33536\n\t" \
  "ds_read_b128 a[144:147], v138 offset:4096\n\t" \
  "buffer_load_dwordx4 a[88:91], v131, %[srdx], s45 offen offset:768\n\t" \
  "s_add_u32 m0, %[m0v], 20480\n\t" \
  "s_add_u32 s44, %[sofv], 16384\n\t" \
  "s_waitcnt lgkmcnt(9)\n\t" \
  "v_mfma_f32_32x32x16_bf16 v[64:79], a[248:251], a[172:175], v[64:79]\n\t" \
  "s_waitcnt vmcnt(13)\n\t" \
  "ds_read_b64_tr_b16 a[200:201], v128 offset:33792\n\t" \
  "ds_read_b128 a[148:151], v139 offset:4096\n\t" \
  "buffer_load_dwordx4 v232, %[srdv], s44 offen nt lds\n\t" \
  "v_mfma_f32_32x32x16_bf16 v[80:95], a[248:251], a[188:191], v[80:95]\n\t" \
  "ds_read_b64_tr_b16 a[202:203], v128 offset:34048\n\t" \
  "ds_read_b128 a[152:155], v140 offset:4096\n\t" \
  "ds_write_b128 v136, a[32:35] offset:0\n\t" \
  "s_waitcnt lgkmcnt(11)\n\t" \
  "v_mfma_f32_32x32x16_bf16 v[96:111], a[252:255], a[172:175], v[96:111]\n\t" \
  "ds_read_b64_tr_b16 a[204:205], v128 offset:34304\n\t" \
  "ds_read_b128 a[156:159], v141 offset:4096\n\t" \
  "buffer_load_dwordx4 v232, %[srdv], s44 offen offset:2048 nt lds\n\t" \
  "s_mov_b32 s45, 57344\n\t" \
  "v_mfma_f32_32x32x16_bf16 v[112:127], a[252:255], a[188:191], v[112:127]\n\t" \
  "ds_read_b64_tr_b16 a[206:207], v128 offset:34560\n\t" \
  "buffer_load_dwordx4 a[92:95], v131, %[srdx], s45 offen offset:768\n\t" \
  "s_waitcnt lgkmcnt(12)\n\t" \
  "v_mfma_f32_32x32x16_bf16 v[0:15], a[192:195], a[128:131], v[0:15]\n\t" \
  "ds_read_b64_tr_b16 a[208:209], v128 offset:36864\n\t" \
  "s_waitcnt lgkmcnt(9)\n\t" \
  "v_mfma_f32_32x32x16_bf16 v[16:31], a[192:195], a[144:147], v[16:31]\n\t" \
  "ds_read_b64_tr_b16 a[210:211], v128 offset:37120\n\t" \
  "ds_write_b128 v137, a[36:39] offset:1024\n\t" \
  "v_mfma_f32_32x32x16_bf16 v[32:47], a[196:199], a[128:131], v[32:47]\n\t" \
  "s_waitcnt vmcnt(24)\n\t" \
  "s_barrier\n\t" \
  "ds_read_b64_tr_b16 a[212:213], v128 offset:37376\n\t" \
  "s_mov_b32 s45, 0\n\t" \
  "v_mfma_f32_32x32x16_bf16 v[48:63], a[196:199], a[144:147], v[48:63]\n\t" \
  "ds_read_b64_tr_b16 a[214:215], v128 offset:37632\n\t" \
  "buffer_load_dwordx4 a[96:99], v131, %[srdx], s45 offen offset:896\n\t" \
  "s_add_u32 m0, %[m0w], 114688\n\t" \
  "s_add_u32 s44, %[sofw], 3670016\n\t" \
  "s_waitcnt lgkmcnt(10)\n\t" \
  "v_mfma_f32_32x32x16_bf16 v[64:79], a[200:203], a[128:131], v[64:79]\n\t" \
  "ds_read_b64_tr_b16 a[216:217], v128 offset:37888\n\t" \
  "buffer_load_dwordx4 v130, %[srdw], s44 offen lds\n\t" \
  "v_mfma_f32_32x32x16_bf16 v[80:95], a[200:203], a[144:147], v[80:95]\n\t" \
  "ds_read_b64_tr_b16 a[218:219], v128 offset:38144\n\t" \
  "ds_write_b128 v136, a[40:43] offset:2048\n\t" \
  "s_waitcnt lgkmcnt(8)\n\t" \
  "v_mfma_f32_32x32x16_bf16 v[96:111], a[204:207], a[128:131], v[96:111]\n\t" \
  "ds_read_b64_tr_b16 a[220:221], v128 offset:38400\n\t" \
  "s_mov_b32 s45, 8192\n\t" \
  "v_mfma_f32_32x32x16_bf16 v[112:127], a[204:207], a[144:147], v[112:127]\n\t" \
  "ds_read_b64_tr_b16 a[222:223], v128 offset:38656\n\t" \
  "buffer_load_dwordx4 a[100:103], v131, %[srdx], s45 offen offset:896\n\t" \
  "s_add_u32 m0, %[m0w], 115712\n\t" \
  "s_add_u32 s44, %[sofw], 3670144\n\t" \
  "s_waitcnt lgkmcnt(8)\n\t" \
  "v_mfma_f32_32x32x16_bf16 v[0:15], a[208:211], a[132:135], v[0:15]\n\t" \
  "ds_read_b64_tr_b16 a[224:225], v128 offset:40960\n\t" \
  "buffer_load_dwordx4 v130, %[srdw], s44 offen lds\n\t" \
  "v_mfma_f32_32x32x16_bf16 v[16:31], a[208:211], a[148:151], v[16:31]\n\t" \
  "ds_read_b64_tr_b16 a[226:227], v128 offset:41216\n\t" \
  "ds_write_b128 v137, a[44:47] offset:3072\n\t" \
  "s_waitcnt lgkmcnt(8)\n\t" \
  "v_mfma_f32_32x32x16_bf16 v[32:47], a[212:215], a[132:135], v[32:47]\n\t" \
  "ds_read_b64_tr_b16 a[228:229], v128 offset:41472\n\t" \
  "s_mov_b32 s45, 16384\n\t" \
  "v_mfma_f32_32x32x16_bf16 v[48:63], a[212:215], a[148:151], v[48:63]\n\t" \
  "ds_read_b64_tr_b16 a[230:231], v128 offset:41728\n\t" \
  "ds_read_b128 a[160:163], v138 offset:0\n\t" \
  "buffer_load_dwordx4 a[104:107], v131, %[srdx], s45 offen offset:896\n\t" \
  "s_add_u32 m0, %[m0w], 116736\n\t" \
  "s_add_u32 s44, %[sofw], 3735552\n\t" \
  "s_waitcnt lgkmcnt(9)\n\t" \
  "v_mfma_f32_32x32x16_bf16 v[64:79], a[216:219], a[132:135], v[64:79]\n\t" \
  "ds_read_b64_tr_b16 a[232:233], v128 offset:41984\n\t" \
  "ds_read_b128 a[164:167], v139 offset:0\n\t" \
  "buffer_load_dwordx4 v130, %[srdw], s44 offen lds\n\t" \
  "v_mfma_f32_32x32x16_bf16 v[80:95], a[216:219], a[148:151], v[80:95]\n\t" \
  "ds_read_b64_tr_b16 a[234:235], v128 offset:42240\n\t" \
  "ds_write_b128 v136, a[48:51] offset:4096\n\t" \
  "ds_read_b128 a[168:171], v140 offset:0\n\t" \
  "s_waitcnt lgkmcnt(11)\n\t" \
  "v_mfma_f32_32x32x16_bf16 v[96:111], a[220:223], a[132:135], v[96:111]\n\t" \
  "ds_read_b64_tr_b16 a[236:237], v128 offset:42496\n\t" \
  "ds_read_b128 a[172:175], v141 offset:0\n\t" \
  "s_mov_b32 s45, 24576\n\t" \
  "v_mfma_f32_32x32x16_bf16 v[112:127], a[220:223], a[148:151], v[112:127]\n\t" \
  "ds_read_b64_tr_b16 a[238:239], v128 offset:42752\n\t" \
  "buffer_load_dwordx4 a[108:111], v131, %[srdx], s45 offen offset:896\n\t" \
  "s_add_u32 m0, %[m0w], 117760\n\t" \
  "s_add_u32 s44, %[sofw], 3735680\n\t" \
  "s_waitcnt lgkmcnt(12)\n\t" \
  "v_mfma_f32_32x32x16_bf16 v[0:15], a[224:227], a[136:139], v[0:15]\n\t" \
  "ds_read_b64_tr_b16 a[240:241], v128 offset:45056\n\t" \
  "buffer_load_dwordx4 v130, %[srdw], s44 offen lds\n\t" \
  "v_mfma_f32_32x32x16_bf16 v[16:31], a[224:227], a[152:155], v[16:31]\n\t" \
  "ds_read_b64_tr_b16 a[242:243], v128 offset:45312\n\t" \
  "ds_write_b128 v137, a[52:55] offset:5120\n\t" \
  "s_waitcnt lgkmcnt(12)\n\t" \
  "v_mfma_f32_32x32x16_bf16 v[32:47], a[228:231], a[136:139], v[32:47]\n\t" \
  "ds_read_b64_tr_b16 a[244:245], v128 offset:45568\n\t" \
  "s_mov_b32 s45, 32768\n\t" \
  "v_mfma_f32_32x32x16_bf16 v[48:63], a[228:231], a[152:155], v[48:63]\n\t" \
  "ds_read_b64_tr_b16 a[246:247], v128 offset:45824\n\t" \
  "buffer_load_dwordx4 a[112:115], v131, %[srdx], s45 offen offset:896\n\t" \
  "s_waitcnt lgkmcnt(10)\n\t" \
  "v_mfma_f32_32x32x16_bf16 v[64:79], a[232:235], a[136:139], v[64:79]\n\t" \
  "ds_read_b64_tr_b16 a[248:249], v128 offset:46080\n\t" \
  "v_mfma_f32_32x32x16_bf16 v[80:95], a[232:235], a[152:155], v[80:95]\n\t" \
  "ds_read_b64_tr_b16 a[250:251], v128 offset:46336\n\t" \
  "ds_write_b128 v136, a[56:59] offset:6144\n\t" \
  "s_waitcnt lgkmcnt(8)\n\t" \
  "v_mfma_f32_32x32x16_bf16 v[96:111], a[236:239], a[136:139], v[96:111]\n\t" \
  "ds_read_b64_tr_b16 a[252:253], v128 offset:46592\n\t" \
  "s_mov_b32 s45, 40960\n\t" \
  "v_mfma_f32_32x32x16_bf16 v[112:127], a[236:239], a[152:155], v[112:127]\n\t" \
  "ds_read_b64_tr_b16 a[254:255], v128 offset:46848\n\t" \
  "buffer_load_dwordx4 a[116:119], v131, %[srdx], s45 offen offset:896\n\t" \
  "s_waitcnt lgkmcnt(8)\n\t" \
  "v_mfma_f32_32x32x16_bf16 v[0:15], a[240:243], a[140:143], v[0:15]\n\t" \
  "ds_read_b64_tr_b16 a[192:193], v129 offset:32768\n\t" \
  "v_mfma_f32_32x32x16_bf16 v[16:31], a[240:243], a[156:159], v[16:31]\n\t" \
  "ds_read_b64_tr_b16 a[194:195], v129 offset:33024\n\t" \
  "ds_write_b128 v137, a[60:63] offset:7168\n\t" \
  "s_waitcnt lgkmcnt(8)\n\t" \
  "v_mfma_f32_32x32x16_bf16 v[32:47], a[244:247], a[140:143], v[32:47]\n\t" \
  "ds_read_b64_tr_b16 a[196:197], v129 offset:33280\n\t" \
  "s_mov_b32 s45, 49152\n\t" \
  "v_mfma_f32_32x32x16_bf16 v[48:63], a[244:247], a[156:159], v[48:63]\n\t" \
  "ds_read_b64_tr_b16 a[198:199], v129 offset:33536\n\t" \
  "ds_read_b128 a[176:179], v138 offset:4096\n\t" \
  "buffer_load_dwordx4 a[120:123], v131, %[srdx], s45 offen offset:896\n\t" \
  "s_waitcnt lgkmcnt(9)\n\t" \
  "v_mfma_f32_32x32x16_bf16 v[64:79], a[248:251], a[140:143], v[64:79]\n\t" \
  "s_waitcnt vmcnt(11)\n\t" \
  "ds_read_b64_tr_b16 a[200:201], v129 offset:33792\n\t" \
  "ds_read_b128 a[180:183], v139 offset:4096\n\t" \
  "v_mfma_f32_32x32x16_bf16 v[80:95], a[248:251], a[156:159], v[80:95]\n\t" \
  "ds_read_b64_tr_b16 a[202:203], v129 offset:34048\n\t" \
  "ds_read_b128 a[184:187], v140 offset:4096\n\t" \
  "ds_write_b128 v136, a[64:67] offset:0\n\t" \
  "s_waitcnt lgkmcnt(11)\n\t" \
  "v_mfma_f32_32x32x16_bf16 v[96:111], a[252:255], a[140:143], v[96:111]\n\t" \
  "ds_read_b64_tr_b16 a[204:205], v129 offset:34304\n\t" \
  "ds_read_b128 a[188:191], v141 offset:4096\n\t" \
  "s_mov_b32 s45, 57344\n\t" \
  "v_mfma_f32_32x32x16_bf16 v[112:127], a[252:255], a[156:159], v[112:127]\n\t" \
  "ds_read_b64_tr_b16 a[206:207], v129 offset:34560\n\t" \
  "buffer_load_dwordx4 a[124:127], v131, %[srdx], s45 offen offset:896\n\t" \
  "s_waitcnt lgkmcnt(12)\n\t" \
  "v_mfma_f32_32x32x16_bf16 v[0:15], a[192:195], a[160:163], v[0:15]\n\t" \
  "ds_read_b64_tr_b16 a[208:209], v129 offset:36864\n\t" \
  "s_waitcnt lgkmcnt(9)\n\t" \
  "v_mfma_f32_32x32x16_bf16 v[16:31], a[192:195], a[176:179], v[16:31]\n\t" \
  "ds_read_b64_tr_b16 a[210:211], v129 offset:37120\n\t" \
  "ds_write_b128 v137, a[68:71] offset:1024\n\t" \
  "v_mfma_f32_32x32x16_bf16 v[32:47], a[196:199], a[160:163], v[32:47]\n\t" \
  "s_waitcnt vmcnt(20)\n\t" \
  "s_barrier\n\t" \
  "ds_read_b64_tr_b16 a[212:213], v129 offset:37376\n\t" \
  "v_mfma_f32_32x32x16_bf16 v[48:63], a[196:199], a[176:179], v[48:63]\n\t" \
  "ds_read_b64_tr_b16 a[214:215], v129 offset:37632\n\t" \
  "s_add_u32 m0, %[m0k], 32768\n\t" \
  "s_add_u32 s44, %[sofk], 32768\n\t" \
  "s_waitcnt lgkmcnt(10)\n\t" \
  "v_mfma_f32_32x32x16_bf16 v[64:79], a[200:203], a[160:163], v[64:79]\n\t" \
  "ds_read_b64_tr_b16 a[216:217], v129 offset:37888\n\t" \
  "buffer_load_dwordx4 v229, %[srdk], s44 offen nt lds\n\t" \
  "v_mfma_f32_32x32x16_bf16 v[80:95], a[200:203], a[176:179], v[80:95]\n\t" \
  "ds_read_b64_tr_b16 a[218:219], v129 offset:38144\n\t" \
  "ds_write_b128 v136, a[72:75] offset:2048\n\t" \
  "s_waitcnt lgkmcnt(8)\n\t" \
  "v_mfma_f32_32x32x16_bf16 v[96:111], a[204:207], a[160:163], v[96:111]\n\t" \
  "ds_read_b64_tr_b16 a[220:221], v129 offset:38400\n\t" \
  "v_mfma_f32_32x32x16_bf16 v[112:127], a[204:207], a[176:179], v[112:127]\n\t" \
  "ds_read_b64_tr_b16 a[222:223], v129 offset:38656\n\t" \
  "s_waitcnt lgkmcnt(8)\n\t" \
  "v_mfma_f32_32x32x16_bf16 v[0:15], a[208:211], a[164:167], v[0:15]\n\t" \
  "ds_read_b64_tr_b16 a[224:225], v129 offset:40960\n\t" \
  "buffer_load_dwordx4 v230, %[srdk], s44 offen offset:1024 nt lds\n\t" \
  "v_mfma_f32_32x32x16_bf16 v[16:31], a[208:211], a[180:183], v[16:31]\n\t" \
  "ds_read_b64_tr_b16 a[226:227], v129 offset:41216\n\t" \
  "ds_write_b128 v137, a[76:79] offset:3072\n\t" \
  "s_waitcnt lgkmcnt(8)\n\t" \
  "v_mfma_f32_32x32x16_bf16 v[32:47], a[212:215], a[164:167], v[32:47]\n\t" \
  "ds_read_b64_tr_b16 a[228:229], v129 offset:41472\n\t" \
  "v_mfma_f32_32x32x16_bf16 v[48:63], a[212:215], a[180:183], v[48:63]\n\t" \
  "ds_read_b64_tr_b16 a[230:231], v129 offset:41728\n\t" \
  "ds_read_b128 a[0:3], v138 offset:0\n\t" \
  "s_waitcnt lgkmcnt(9)\n\t" \
  "v_mfma_f32_32x32x16_bf16 v[64:79], a[216:219], a[164:167], v[64:79]\n\t" \
  "ds_read_b64_tr_b16 a[232:233], v129 offset:41984\n\t" \
  "ds_read_b128 a[4:7], v139 offset:0\n\t" \
  "buffer_load_dwordx4 v229, %[srdk], s44 offen offset:2048 nt lds\n\t" \
  "v_mfma_f32_32x32x16_bf16 v[80:95], a[216:219], a[180:183], v[80:95]\n\t" \
  "ds_read_b64_tr_b16 a[234:235], v129 offset:42240\n\t" \
  "ds_write_b128 v136, a[80:83] offset:4096\n\t" \
  "ds_read_b128 a[8:11], v140 offset:0\n\t" \
  "s_waitcnt lgkmcnt(11)\n\t" \
  "v_mfma_f32_32x32x16_bf16 v[96:111], a[220:223], a[164:167], v[96:111]\n\t" \
  "ds_read_b64_tr_b16 a[236:237], v129 offset:42496\n\t" \
  "ds_read_b128 a[12:15], v141 offset:0\n\t" \
  "v_mfma_f32_32x32x16_bf16 v[112:127], a[220:223], a[180:183], v[112:127]\n\t" \
  "ds_read_b64_tr_b16 a[238:239], v129 offset:42752\n\t" \
  "s_waitcnt lgkmcnt(12)\n\t" \
  "v_mfma_f32_32x32x16_bf16 v[0:15], a[224:227], a[168:171], v[0:15]\n\t" \
  "ds_read_b64_tr_b16 a[240:241], v129 offset:45056\n\t" \
  "buffer_load_dwordx4 v230, %[srdk], s44 offen offset:3072 nt lds\n\t" \
  "v_mfma_f32_32x32x16_bf16 v[16:31], a[224:227], a[184:187], v[16:31]\n\t" \
  "ds_read_b64_tr_b16 a[242:243], v129 offset:45312\n\t" \
  "ds_write_b128 v137, a[84:87] offset:5120\n\t" \
  "s_waitcnt lgkmcnt(12)\n\t" \
  "v_mfma_f32_32x32x16_bf16 v[32:47], a[228:231], a[168:171], v[32:47]\n\t" \
  "ds_read_b64_tr_b16 a[244:245], v129 offset:45568\n\t" \
  "v_mfma_f32_32x32x16_bf16 v[48:63], a[228:231], a[184:187], v[48:63]\n\t" \
  "ds_read_b64_tr_b16 a[246:247], v129 offset:45824\n\t" \
  "s_waitcnt lgkmcnt(10)\n\t" \
  "v_mfma_f32_32x32x16_bf16 v[64:79], a[232:235], a[168:171], v[64:79]\n\t" \
  "ds_read_b64_tr_b16 a[248:249], v129 offset:46080\n\t" \
  "v_mfma_f32_32x32x16_bf16 v[80:95], a[232:235], a[184:187], v[80:95]\n\t" \
  "ds_read_b64_tr_b16 a[250:251], v129 offset:46336\n\t" \
  "ds_write_b128 v136, a[88:91] offset:6144\n\t" \
  "s_waitcnt lgkmcnt(8)\n\t" \
  "v_mfma_f32_32x32x16_bf16 v[96:111], a[236:239], a[168:171], v[96:111]\n\t" \
  "ds_read_b64_tr_b16 a[252:253], v129 offset:46592\n\t" \
  "v_mfma_f32_32x32x16_bf16 v[112:127], a[236:239], a[184:187], v[112:127]\n\t" \
  "ds_read_b64_tr_b16 a[254:255], v129 offset:46848\n\t" \
  "s_waitcnt lgkmcnt(8)\n\t" \
  "v_mfma_f32_32x32x16_bf16 v[0:15], a[240:243], a[172:175], v[0:15]\n\t" \
  "ds_read_b64_tr_b16 a[192:193], v128 offset:49152\n\t" \
  "v_mfma_f32_32x32x16_bf16 v[16:31], a[240:243], a[188:191], v[16:31]\n\t" \
  "ds_read_b64_tr_b16 a[194:195], v128 offset:49408\n\t" \
  "ds_write_b128 v137, a[92:95] offset:7168\n\t" \
  "s_waitcnt lgkmcnt(8)\n\t" \
  "v_mfma_f32_32x32x16_bf16 v[32:47], a[244:247], a[172:175], v[32:47]\n\t" \
  "ds_read_b64_tr_b16 a[196:197], v128 offset:49664\n\t" \
  "v_mfma_f32_32x32x16_bf16 v[48:63], a[244:247], a[188:191], v[48:63]\n\t" \
  "ds_read_b64_tr_b16 a[198:199], v128 offset:49920\n\t" \
  "ds_read_b128 a[16:19], v138 offset:4096\n\t" \
  "s_waitcnt lgkmcnt(9)\n\t" \
  "v_mfma_f32_32x32x16_bf16 v[64:79], a[248:251], a[172:175], v[64:79]\n\t" \
  "s_waitcnt vmcnt(4)\n\t" \
  "ds_read_b64_tr_b16 a[200:201], v128 offset:50176\n\t" \
  "ds_read_b128 a[20:23], v139 offset:4096\n\t" \
  "v_mfma_f32_32x32x16_bf16 v[80:95], a[248:251], a[188:191], v[80:95]\n\t" \
  "ds_read_b64_tr_b16 a[202:203], v128 offset:50432\n\t" \
  "ds_read_b128 a[24:27], v140 offset:4096\n\t" \
  "ds_write_b128 v136, a[96:99] offset:0\n\t" \
  "s_waitcnt lgkmcnt(11)\n\t" \
  "v_mfma_f32_32x32x16_bf16 v[96:111], a[252:255], a[172:175], v[96:111]\n\t" \
  "ds_read_b64_tr_b16 a[204:205], v128 offset:50688\n\t" \
  "ds_read_b128 a[28:31], v141 offset:4096\n\t" \
  "v_mfma_f32_32x32x16_bf16 v[112:127], a[252:255], a[188:191], v[112:127]\n\t" \
  "ds_read_b64_tr_b16 a[206:207], v128 offset:50944\n\t" \
  "s_waitcnt lgkmcnt(12)\n\t" \
  "v_mfma_f32_32x32x16_bf16 v[0:15], a[192:195], a[0:3], v[0:15]\n\t" \
  "ds_read_b64_tr_b16 a[208:209], v128 offset:53248\n\t" \
  "s_waitcnt lgkmcnt(9)\n\t" \
  "v_mfma_f32_32x32x16_bf16 v[16:31], a[192:195], a[16:19], v[16:31]\n\t" \
  "ds_read_b64_tr_b16 a[210:211], v128 offset:53504\n\t" \
  "ds_write_b128 v137, a[100:103] offset:1024\n\t" \
  "v_mfma_f32_32x32x16_bf16 v[32:47], a[196:199], a[0:3], v[32:47]\n\t" \
  "s_waitcnt vmcnt(8)\n\t" \
  "s_barrier\n\t" \
  "ds_read_b64_tr_b16 a[212:213], v128 offset:53760\n\t" \
  "v_mfma_f32_32x32x16_bf16 v[48:63], a[196:199], a[16:19], v[48:63]\n\t" \
  "ds_read_b64_tr_b16 a[214:215], v128 offset:54016\n\t" \
  "s_add_u32 m0, %[m0v], 32768\n\t" \
  "s_add_u32 s44, %[sofv], 32768\n\t" \
  "s_waitcnt lgkmcnt(10)\n\t" \
  "v_mfma_f32_32x32x16_bf16 v[64:79], a[200:203], a[0:3], v[64:79]\n\t" \
  "ds_read_b64_tr_b16 a[216:217], v128 offset:54272\n\t" \
  "buffer_load_dwordx4 v231, %[srdv], s44 offen nt lds\n\t" \
  "v_mfma_f32_32x32x16_bf16 v[80:95], a[200:203], a[16:19], v[80:95]\n\t" \
  "ds_read_b64_tr_b16 a[218:219], v128 offset:54528\n\t" \
  "ds_write_b128 v136, a[104:107] offset:2048\n\t" \
  "s_waitcnt lgkmcnt(8)\n\t" \
  "v_mfma_f32_32x32x16_bf16 v[96:111], a[204:207], a[0:3], v[96:111]\n\t" \
  "ds_read_b64_tr_b16 a[220:221], v128 offset:54784\n\t" \
  "v_mfma_f32_32x32x16_bf16 v[112:127], a[204:207], a[16:19], v[112:127]\n\t" \
  "ds_read_b64_tr_b16 a[222:223], v128 offset:55040\n\t" \
  "s_waitcnt lgkmcnt(8)\n\t" \
  "v_mfma_f32_32x32x16_bf16 v[0:15], a[208:211], a[4:7], v[0:15]\n\t" \
  "ds_read_b64_tr_b16 a[224:225], v128 offset:57344\n\t" \
  "buffer_load_dwordx4 v231, %[srdv], s44 offen offset:2048 nt lds\n\t" \
  "v_mfma_f32_32x32x16_bf16 v[16:31], a[208:211], a[20:23], v[16:31]\n\t" \
  "ds_read_b64_tr_b16 a[226:227], v128 offset:57600\n\t" \
  "ds_write_b128 v137, a[108:111] offset:3072\n\t" \
  "s_waitcnt lgkmcnt(8)\n\t" \
  "v_mfma_f32_32x32x16_bf16 v[32:47], a[212:215], a[4:7], v[32:47]\n\t" \
  "ds_read_b64_tr_b16 a[228:229], v128 offset:57856\n\t" \
  "v_mfma_f32_32x32x16_bf16 v[48:63], a[212:215], a[20:23], v[48:63]\n\t" \
  "ds_read_b64_tr_b16 a[230:231], v128 offset:58112\n\t" \
  "ds_read_b128 a[32:35], v138 offset:0\n\t" \
  "s_add_u32 m0, %[m0v], 36864\n\t" \
  "s_add_u32 s44, %[sofv], 32768\n\t" \
  "s_waitcnt lgkmcnt(9)\n\t" \
  "v_mfma_f32_32x32x16_bf16 v[64:79], a[216:219], a[4:7], v[64:79]\n\t" \
  "ds_read_b64_tr_b16 a[232:233], v128 offset:58368\n\t" \
  "ds_read_b128 a[36:39], v139 offset:0\n\t" \
  "buffer_load_dwordx4 v232, %[srdv], s44 offen nt lds\n\t" \
  "v_mfma_f32_32x32x16_bf16 v[80:95], a[216:219], a[20:23], v[80:95]\n\t" \
  "ds_read_b64_tr_b16 a[234:235], v128 offset:58624\n\t" \
  "ds_write_b128 v136, a[112:115] offset:4096\n\t" \
  "ds_read_b128 a[40:43], v140 offset:0\n\t" \
  "s_waitcnt lgkmcnt(11)\n\t" \
  "v_mfma_f32_32x32x16_bf16 v[96:111], a[220:223], a[4:7], v[96:111]\n\t" \
  "ds_read_b64_tr_b16 a[236:237], v128 offset:58880\n\t" \
  "ds_read_b128 a[44:47], v141 offset:0\n\t" \
  "v_mfma_f32_32x32x16_bf16 v[112:127], a[220:223], a[20:23], v[112:127]\n\t" \
  "ds_read_b64_tr_b16 a[238:239], v128 offset:59136\n\t" \
  "s_waitcnt lgkmcnt(12)\n\t" \
  "v_mfma_f32_32x32x16_bf16 v[0:15], a[224:227], a[8:11], v[0:15]\n\t" \
  "ds_read_b64_tr_b16 a[240:241], v128 offset:61440\n\t" \
  "buffer_load_dwordx4 v232, %[srdv], s44 offen offset:2048 nt lds\n\t" \
  "v_mfma_f32_32x32x16_bf16 v[16:31], a[224:227], a[24:27], v[16:31]\n\t" \
  "ds_read_b64_tr_b16 a[242:243], v128 offset:61696\n\t" \
  "ds_write_b128 v137, a[116:119] offset:5120\n\t" \
  "s_waitcnt lgkmcnt(12)\n\t" \
  "v_mfma_f32_32x32x16_bf16 v[32:47], a[228:231], a[8:11], v[32:47]\n\t" \
  "ds_read_b64_tr_b16 a[244:245], v128 offset:61952\n\t" \
  "v_mfma_f32_32x32x16_bf16 v[48:63], a[228:231], a[24:27], v[48:63]\n\t" \
  "ds_read_b64_tr_b16 a[246:247], v128 offset:62208\n\t" \
  "s_waitcnt lgkmcnt(10)\n\t" \
  "v_mfma_f32_32x32x16_bf16 v[64:79], a[232:235], a[8:11], v[64:79]\n\t" \
  "ds_read_b64_tr_b16 a[248:249], v128 offset:62464\n\t" \
  "v_mfma_f32_32x32x16_bf16 v[80:95], a[232:235], a[24:27], v[80:95]\n\t" \
  "ds_read_b64_tr_b16 a[250:251], v128 offset:62720\n\t" \
  "ds_write_b128 v136, a[120:123] offset:6144\n\t" \
  "s_waitcnt lgkmcnt(8)\n\t" \
  "v_mfma_f32_32x32x16_bf16 v[96:111], a[236:239], a[8:11], v[96:111]\n\t" \
  "ds_read_b64_tr_b16 a[252:253], v128 offset:62976\n\t" \
  "v_mfma_f32_32x32x16_bf16 v[112:127], a[236:239], a[24:27], v[112:127]\n\t" \
  "ds_read_b64_tr_b16 a[254:255], v128 offset:63232\n\t" \
  "s_waitcnt lgkmcnt(8)\n\t" \
  "v_mfma_f32_32x32x16_bf16 v[0:15], a[240:243], a[12:15], v[0:15]\n\t" \
  "ds_read_b64_tr_b16 a[192:193], v129 offset:49152\n\t" \
  "v_mfma_f32_32x32x16_bf16 v[16:31], a[240:243], a[28:31], v[16:31]\n\t" \
  "ds_read_b64_tr_b16 a[194:195], v129 offset:49408\n\t" \
  "ds_write_b128 v137, a[124:127] offset:7168\n\t" \
  "s_waitcnt lgkmcnt(8)\n\t" \
  "v_mfma_f32_32x32x16_bf16 v[32:47], a[244:247], a[12:15], v[32:47]\n\t" \
  "ds_read_b64_tr_b16 a[208:209], v129 offset:53248\n\t" \
  "v_mfma_f32_32x32x16_bf16 v[48:63], a[244:247], a[28:31], v[48:63]\n\t" \
  "ds_read_b64_tr_b16 a[210:211], v129 offset:53504\n\t" \
  "ds_read_b128 a[48:51], v138 offset:4096\n\t" \
  "s_waitcnt lgkmcnt(9)\n\t" \
  "v_mfma_f32_32x32x16_bf16 v[64:79], a[248:251], a[12:15], v[64:79]\n\t" \
  "ds_read_b64_tr_b16 a[224:225], v129 offset:57344\n\t" \
  "ds_read_b128 a[52:55], v139 offset:4096\n\t" \
  "v_mfma_f32_32x32x16_bf16 v[80:95], a[248:251], a[28:31], v[80:95]\n\t" \
  "ds_read_b64_tr_b16 a[226:227], v129 offset:57600\n\t" \
  "ds_read_b128 a[56:59], v140 offset:4096\n\t" \
  "s_waitcnt lgkmcnt(10)\n\t" \
  "v_mfma_f32_32x32x16_bf16 v[96:111], a[252:255], a[12:15], v[96:111]\n\t" \
  "ds_read_b64_tr_b16 a[240:241], v129 offset:61440\n\t" \
  "ds_read_b128 a[60:63], v141 offset:4096\n\t" \
  "v_mfma_f32_32x32x16_bf16 v[112:127], a[252:255], a[28:31], v[112:127]\n\t" \
  "ds_read_b64_tr_b16 a[242:243], v129 offset:61696\n\t" \
  "s_waitcnt lgkmcnt(11)\n\t" \
  "v_mfma_f32_32x32x16_bf16 v[0:15], a[192:195], a[32:35], v[0:15]\n\t" \
  "ds_read_b64_tr_b16 a[196:197], v129 offset:49664\n\t" \
  "s_waitcnt lgkmcnt(8)\n\t" \
  "v_mfma_f32_32x32x16_bf16 v[16:31], a[192:195], a[48:51], v[16:31]\n\t" \
  "ds_read_b64_tr_b16 a[198:199], v129 offset:49920\n\t" \
  "v_mfma_f32_32x32x16_bf16 v[0:15], a[208:211], a[36:39], v[0:15]\n\t" \
  "s_waitcnt vmcnt(63)\n\t" \
  "s_barrier\n\t" \
  "ds_read_b64_tr_b16 a[212:213], v129 offset:53760\n\t" \
  "s_waitcnt lgkmcnt(8)\n\t" \
  "v_mfma_f32_32x32x16_bf16 v[16:31], a[208:211], a[52:55], v[16:31]\n\t" \
  "ds_read_b64_tr_b16 a[214:215], v129 offset:54016\n\t" \
  "ds_read_b128 a[192:195], v224 offset:0\n\t" \
  "ds_read_b128 a[208:211], v224 offset:128\n\t" \
  "s_add_u32 m0, %[m0k], 49152\n\t" \
  "s_add_u32 s44, %[sofk], 49152\n\t" \
  "s_waitcnt lgkmcnt(10)\n\t" \
  "v_mfma_f32_32x32x16_bf16 v[0:15], a[224:227], a[40:43], v[0:15]\n\t" \
  "ds_read_b64_tr_b16 a[228:229], v129 offset:57856\n\t" \
  "buffer_load_dwordx4 v229, %[srdk], s44 offen nt lds\n\t" \
  "s_waitcnt lgkmcnt(10)\n\t" \
  "v_mfma_f32_32x32x16_bf16 v[16:31], a[224:227], a[56:59], v[16:31]\n\t" \
  "ds_read_b64_tr_b16 a[230:231], v129 offset:58112\n\t" \
  "ds_read_b128 a[224:227], v224 offset:8192\n\t" \
  "s_waitcnt lgkmcnt(9)\n\t" \
  "v_mfma_f32_32x32x16_bf16 v[0:15], a[240:243], a[44:47], v[0:15]\n\t" \
  "ds_read_b64_tr_b16 a[244:245], v129 offset:61952\n\t" \
  "v_mfma_f32_32x32x16_bf16 v[16:31], a[240:243], a[60:63], v[16:31]\n\t" \
  "ds_read_b64_tr_b16 a[246:247], v129 offset:62208\n\t" \
  "ds_read_b128 a[240:243], v224 offset:8320\n\t" \
  "s_waitcnt lgkmcnt(10)\n\t" \
  "v_mfma_f32_32x32x16_bf16 v[32:47], a[196:199], a[32:35], v[32:47]\n\t" \
  "ds_read_b64_tr_b16 a[200:201], v129 offset:50176\n\t" \
  "buffer_load_dwordx4 v230, %[srdk], s44 offen offset:1024 nt lds\n\t" \
  "v_mfma_f32_32x32x16_bf16 v[48:63], a[196:199], a[48:51], v[48:63]\n\t" \
  "ds_read_b64_tr_b16 a[202:203], v129 offset:50432\n\t" \
  "ds_read_b128 a[196:199], v225 offset:0\n\t" \
  "s_waitcnt lgkmcnt(11)\n\t" \
  "v_mfma_f32_32x32x16_bf16 v[32:47], a[212:215], a[36:39], v[32:47]\n\t" \
  "ds_read_b64_tr_b16 a[216:217], v129 offset:54272\n\t" \
  "v_mul_f32_e32 v0, 0x3e0293ee, v0\n\t" \
  "v_mul_f32_e32 v1, 0x3e0293ee, v1\n\t" \
  "v_mul_f32_e32 v2, 0x3e0293ee, v2\n\t" \
  "v_mul_f32_e32 v3, 0x3e0293ee, v3\n\t" \
  "v_mul_f32_e32 v4, 0x3e0293ee, v4\n\t" \
  "v_mul_f32_e32 v5, 0x3e0293ee, v5\n\t" \
  "v_mul_f32_e32 v6, 0x3e0293ee, v6\n\t" \
  "v_mul_f32_e32 v7, 0x3e0293ee, v7\n\t" \
  "v_cvt_pk_bf16_f32 v0, v0, v1\n\t" \
  "v_mfma_f32_32x32x16_bf16 v[48:63], a[212:215], a[52:55], v[48:63]\n\t" \
  "ds_read_b64_tr_b16 a[218:219], v129 offset:54528\n\t" \
  "ds_read_b128 a[212:215], v225 offset:128\n\t" \
  "v_cvt_pk_bf16_f32 v1, v2, v3\n\t" \
  "v_cvt_pk_bf16_f32 v2, v4, v5\n\t" \
  "v_cvt_pk_bf16_f32 v3, v6, v7\n\t" \
  "v_mul_f32_e32 v8, 0x3e0293ee, v8\n\t" \
  "v_mul_f32_e32 v9, 0x3e0293ee, v9\n\t" \
  "v_mul_f32_e32 v10, 0x3e0293ee, v10\n\t" \
  "v_mul_f32_e32 v11, 0x3e0293ee, v11\n\t" \
  "v_mul_f32_e32 v12, 0x3e0293ee, v12\n\t" \
  "v_mul_f32_e32 v13, 0x3e0293ee, v13\n\t" \
  "s_waitcnt lgkmcnt(10)\n\t" \
  "v_mfma_f32_32x32x16_bf16 v[32:47], a[228:231], a[40:43], v[32:47]\n\t" \
  "ds_read_b64_tr_b16 a[232:233], v129 offset:58368\n\t" \
  "buffer_load_dwordx4 v229, %[srdk], s44 offen offset:2048 nt lds\n\t" \
  "v_mul_f32_e32 v14, 0x3e0293ee, v14\n\t" \
  "v_mul_f32_e32 v15, 0x3e0293ee, v15\n\t" \
  "v_cvt_pk_bf16_f32 v8, v8, v9\n\t" \
  "v_cvt_pk_bf16_f32 v9, v10, v11\n\t" \
  "v_cvt_pk_bf16_f32 v10, v12, v13\n\t" \
  "v_cvt_pk_bf16_f32 v11, v14, v15\n\t" \
  "v_permlane32_swap_b32_e32 v0, v2\n\t" \
  "v_permlane32_swap_b32_e32 v1, v3\n\t" \
  "v_permlane32_swap_b32_e32 v8, v10\n\t" \
  "v_mfma_f32_32x32x16_bf16 v[48:63], a[228:231], a[56:59], v[48:63]\n\t" \
  "ds_read_b64_tr_b16 a[234:235], v129 offset:58624\n\t" \
  "ds_read_b128 a[228:231], v225 offset:8192\n\t" \
  "v_permlane32_swap_b32_e32 v9, v11\n\t" \
  "v_accvgpr_write_b32 a128, v0\n\t" \
  "v_accvgpr_write_b32 a129, v1\n\t" \
  "v_accvgpr_write_b32 a130, v2\n\t" \
  "v_accvgpr_write_b32 a131, v3\n\t" \
  "v_accvgpr_write_b32 a132, v8\n\t" \
  "v_accvgpr_write_b32 a133, v9\n\t" \
  "v_accvgpr_write_b32 a134, v10\n\t" \
  "v_accvgpr_write_b32 a135, v11\n\t" \
  "s_waitcnt lgkmcnt(10)\n\t" \
  "v_mfma_f32_32x32x16_bf16 v[32:47], a[244:247], a[44:47], v[32:47]\n\t" \
  "ds_read_b64_tr_b16 a[248:249], v129 offset:62464\n\t" \
  "v_mul_f32_e32 v16, 0x3e0293ee, v16\n\t" \
  "v_mul_f32_e32 v17, 0x3e0293ee, v17\n\t" \
  "v_mul_f32_e32 v18, 0x3e0293ee, v18\n\t" \
  "v_mul_f32_e32 v19, 0x3e0293ee, v19\n\t" \
  "v_mul_f32_e32 v20, 0x3e0293ee, v20\n\t" \
  "v_mul_f32_e32 v21, 0x3e0293ee, v21\n\t" \
  "v_mul_f32_e32 v22, 0x3e0293ee, v22\n\t" \
  "v_mul_f32_e32 v23, 0x3e0293ee, v23\n\t" \
  "v_cvt_pk_bf16_f32 v16, v16, v17\n\t" \
  "v_mfma_f32_32x32x16_bf16 v[48:63], a[244:247], a[60:63], v[48:63]\n\t" \
  "ds_read_b64_tr_b16 a[250:251], v129 offset:62720\n\t" \
  "ds_read_b128 a[244:247], v225 offset:8320\n\t" \
  "v_cvt_pk_bf16_f32 v17, v18, v19\n\t" \
  "v_cvt_pk_bf16_f32 v18, v20, v21\n\t" \
  "v_cvt_pk_bf16_f32 v19, v22, v23\n\t" \
  "v_mul_f32_e32 v24, 0x3e0293ee, v24\n\t" \
  "v_mul_f32_e32 v25, 0x3e0293ee, v25\n\t" \
  "v_mul_f32_e32 v26, 0x3e0293ee, v26\n\t" \
  "v_mul_f32_e32 v27, 0x3e0293ee, v27\n\t" \
  "v_mul_f32_e32 v28, 0x3e0293ee, v28\n\t" \
  "v_mul_f32_e32 v29, 0x3e0293ee, v29\n\t" \
  "s_waitcnt lgkmcnt(10)\n\t" \
  "v_mfma_f32_32x32x16_bf16 v[64:79], a[200:203], a[32:35], v[64:79]\n\t" \
  "ds_read_b64_tr_b16 a[204:205], v129 offset:50688\n\t" \
  "buffer_load_dwordx4 v230, %[srdk], s44 offen offset:3072 nt lds\n\t" \
  "v_mul_f32_e32 v30, 0x3e0293ee, v30\n\t" \
  "v_mul_f32_e32 v31, 0x3e0293ee, v31\n\t" \
  "v_cvt_pk_bf16_f32 v24, v24, v25\n\t" \
  "v_cvt_pk_bf16_f32 v25, v26, v27\n\t" \
  "v_cvt_pk_bf16_f32 v26, v28, v29\n\t" \
  "v_cvt_pk_bf16_f32 v27, v30, v31\n\t" \
  "v_permlane32_swap_b32_e32 v16, v18\n\t" \
  "v_permlane32_swap_b32_e32 v17, v19\n\t" \
  "v_permlane32_swap_b32_e32 v24, v26\n\t" \
  "v_mfma_f32_32x32x16_bf16 v[80:95], a[200:203], a[48:51], v[80:95]\n\t" \
  "ds_read_b64_tr_b16 a[206:207], v129 offset:50944\n\t" \
  "ds_read_b128 a[200:203], v226 offset:0\n\t" \
  "v_permlane32_swap_b32_e32 v25, v27\n\t" \
  "v_accvgpr_write_b32 a160, v16\n\t" \
  "v_accvgpr_write_b32 a161, v17\n\t" \
  "v_accvgpr_write_b32 a162, v18\n\t" \
  "v_accvgpr_write_b32 a163, v19\n\t" \
  "v_accvgpr_write_b32 a164, v24\n\t" \
  "v_accvgpr_write_b32 a165, v25\n\t" \
  "v_accvgpr_write_b32 a166, v26\n\t" \
  "v_accvgpr_write_b32 a167, v27\n\t" \
  "s_waitcnt lgkmcnt(10)\n\t" \
  "v_mfma_f32_32x32x16_bf16 v[64:79], a[216:219], a[36:39], v[64:79]\n\t" \
  "ds_read_b64_tr_b16 a[220:221], v129 offset:54784\n\t" \
  "v_mul_f32_e32 v32, 0x3e0293ee, v32\n\t" \
  "v_mul_f32_e32 v33, 0x3e0293ee, v33\n\t" \
  "v_mul_f32_e32 v34, 0x3e0293ee, v34\n\t" \
  "v_mul_f32_e32 v35, 0x3e0293ee, v35\n\t" \
  "v_mul_f32_e32 v36, 0x3e0293ee, v36\n\t" \
  "v_mul_f32_e32 v37, 0x3e0293ee, v37\n\t" \
  "v_mul_f32_e32 v38, 0x3e0293ee, v38\n\t" \
  "v_mul_f32_e32 v39, 0x3e0293ee, v39\n\t" \
  "v_cvt_pk_bf16_f32 v32, v32, v33\n\t" \
  "v_mfma_f32_32x32x16_bf16 v[80:95], a[216:219], a[52:55], v[80:95]\n\t" \
  "ds_read_b64_tr_b16 a[222:223], v129 offset:55040\n\t" \
  "ds_read_b128 a[216:219], v226 offset:128\n\t" \
  "v_cvt_pk_bf16_f32 v33, v34, v35\n\t" \
  "v_cvt_pk_bf16_f32 v34, v36, v37\n\t" \
  "v_cvt_pk_bf16_f32 v35, v38, v39\n\t" \
  "v_mul_f32_e32 v40, 0x3e0293ee, v40\n\t" \
  "v_mul_f32_e32 v41, 0x3e0293ee, v41\n\t" \
  "v_mul_f32_e32 v42, 0x3e0293ee, v42\n\t" \
  "v_mul_f32_e32 v43, 0x3e0293ee, v43\n\t" \
  "v_mul_f32_e32 v44, 0x3e0293ee, v44\n\t" \
  "v_mul_f32_e32 v45, 0x3e0293ee, v45\n\t" \
  "s_waitcnt lgkmcnt(10)\n\t" \
  "v_mfma_f32_32x32x16_bf16 v[64:79], a[232:235], a[40:43], v[64:79]\n\t" \
  "ds_read_b64_tr_b16 a[236:237], v129 offset:58880\n\t" \
  "v_mul_f32_e32 v46, 0x3e0293ee, v46\n\t" \
  "v_mul_f32_e32 v47, 0x3e0293ee, v47\n\t" \
  "v_cvt_pk_bf16_f32 v40, v40, v41\n\t" \
  "v_cvt_pk_bf16_f32 v41, v42, v43\n\t" \
  "v_cvt_pk_bf16_f32 v42, v44, v45\n\t" \
  "v_cvt_pk_bf16_f32 v43, v46, v47\n\t" \
  "v_permlane32_swap_b32_e32 v32, v34\n\t" \
  "v_permlane32_swap_b32_e32 v33, v35\n\t" \
  "v_permlane32_swap_b32_e32 v40, v42\n\t" \
  "v_mfma_f32_32x32x16_bf16 v[80:95], a[232:235], a[56:59], v[80:95]\n\t" \
  "ds_read_b64_tr_b16 a[238:239], v129 offset:59136\n\t" \
  "ds_read_b128 a[232:235], v226 offset:8192\n\t" \
  "v_permlane32_swap_b32_e32 v41, v43\n\t" \
  "v_accvgpr_write_b32 a136, v32\n\t" \
  "v_accvgpr_write_b32 a137, v33\n\t" \
  "v_accvgpr_write_b32 a138, v34\n\t" \
  "v_accvgpr_write_b32 a139, v35\n\t" \
  "v_accvgpr_write_b32 a140, v40\n\t" \
  "v_accvgpr_write_b32 a141, v41\n\t" \
  "v_accvgpr_write_b32 a142, v42\n\t" \
  "v_accvgpr_write_b32 a143, v43\n\t" \
  "s_waitcnt lgkmcnt(10)\n\t" \
  "v_mfma_f32_32x32x16_bf16 v[64:79], a[248:251], a[44:47], v[64:79]\n\t" \
  "ds_read_b64_tr_b16 a[252:253], v129 offset:62976\n\t" \
  "v_mul_f32_e32 v48, 0x3e0293ee, v48\n\t" \
  "v_mul_f32_e32 v49, 0x3e0293ee, v49\n\t" \
  "v_mul_f32_e32 v50, 0x3e0293ee, v50\n\t" \
  "v_mul_f32_e32 v51, 0x3e0293ee, v51\n\t" \
  "v_mul_f32_e32 v52, 0x3e0293ee, v52\n\t" \
  "v_mul_f32_e32 v53, 0x3e0293ee, v53\n\t" \
  "v_mul_f32_e32 v54, 0x3e0293ee, v54\n\t" \
  "v_mul_f32_e32 v55, 0x3e0293ee, v55\n\t" \
  "v_cvt_pk_bf16_f32 v48, v48, v49\n\t" \
  "v_mfma_f32_32x32x16_bf16 v[80:95], a[248:251], a[60:63], v[80:95]\n\t" \
  "ds_read_b64_tr_b16 a[254:255], v129 offset:63232\n\t" \
  "ds_read_b128 a[248:251], v226 offset:8320\n\t" \
  "v_cvt_pk_bf16_f32 v49, v50, v51\n\t" \
  "v_cvt_pk_bf16_f32 v50, v52, v53\n\t" \
  "v_cvt_pk_bf16_f32 v51, v54, v55\n\t" \
  "v_mul_f32_e32 v56, 0x3e0293ee, v56\n\t" \
  "v_mul_f32_e32 v57, 0x3e0293ee, v57\n\t" \
  "v_mul_f32_e32 v58, 0x3e0293ee, v58\n\t" \
  "v_mul_f32_e32 v59, 0x3e0293ee, v59\n\t" \
  "v_mul_f32_e32 v60, 0x3e0293ee, v60\n\t" \
  "v_mul_f32_e32 v61, 0x3e0293ee, v61\n\t" \
  "s_waitcnt lgkmcnt(10)\n\t" \
  "v_mfma_f32_32x32x16_bf16 v[96:111], a[204:207], a[32:35], v[96:111]\n\t" \
  "v_mul_f32_e32 v62, 0x3e0293ee, v62\n\t" \
  "v_mul_f32_e32 v63, 0x3e0293ee, v63\n\t" \
  "v_cvt_pk_bf16_f32 v56, v56, v57\n\t" \
  "v_cvt_pk_bf16_f32 v57, v58, v59\n\t" \
  "v_cvt_pk_bf16_f32 v58, v60, v61\n\t" \
  "v_cvt_pk_bf16_f32 v59, v62, v63\n\t" \
  "v_permlane32_swap_b32_e32 v48, v50\n\t" \
  "v_permlane32_swap_b32_e32 v49, v51\n\t" \
  "v_permlane32_swap_b32_e32 v56, v58\n\t" \
  "v_mfma_f32_32x32x16_bf16 v[112:127], a[204:207], a[48:51], v[112:127]\n\t" \
  "ds_read_b128 a[204:207], v227 offset:0\n\t" \
  "v_permlane32_swap_b32_e32 v57, v59\n\t" \
  "v_accvgpr_write_b32 a168, v48\n\t" \
  "v_accvgpr_write_b32 a169, v49\n\t" \
  "v_accvgpr_write_b32 a170, v50\n\t" \
  "v_accvgpr_write_b32 a171, v51\n\t" \
  "v_accvgpr_write_b32 a172, v56\n\t" \
  "v_accvgpr_write_b32 a173, v57\n\t" \
  "v_accvgpr_write_b32 a174, v58\n\t" \
  "v_accvgpr_write_b32 a175, v59\n\t" \
  "s_waitcnt lgkmcnt(8)\n\t" \
  "v_mfma_f32_32x32x16_bf16 v[96:111], a[220:223], a[36:39], v[96:111]\n\t" \
  "v_mul_f32_e32 v64, 0x3e0293ee, v64\n\t" \
  "v_mul_f32_e32 v65, 0x3e0293ee, v65\n\t" \
  "v_mul_f32_e32 v66, 0x3e0293ee, v66\n\t" \
  "v_mul_f32_e32 v67, 0x3e0293ee, v67\n\t" \
  "v_mul_f32_e32 v68, 0x3e0293ee, v68\n\t" \
  "v_mul_f32_e32 v69, 0x3e0293ee, v69\n\t" \
  "v_mul_f32_e32 v70, 0x3e0293ee, v70\n\t" \
  "v_mul_f32_e32 v71, 0x3e0293ee, v71\n\t" \
  "v_cvt_pk_bf16_f32 v64, v64, v65\n\t" \
  "v_mfma_f32_32x32x16_bf16 v[112:127], a[220:223], a[52:55], v[112:127]\n\t" \
  "ds_read_b128 a[220:223], v227 offset:128\n\t" \
  "v_cvt_pk_bf16_f32 v65, v66, v67\n\t" \
  "v_cvt_pk_bf16_f32 v66, v68, v69\n\t" \
  "v_cvt_pk_bf16_f32 v67, v70, v71\n\t" \
  "v_mul_f32_e32 v72, 0x3e0293ee, v72\n\t" \
  "v_mul_f32_e32 v73, 0x3e0293ee, v73\n\t" \
  "v_mul_f32_e32 v74, 0x3e0293ee, v74\n\t" \
  "v_mul_f32_e32 v75, 0x3e0293ee, v75\n\t" \
  "v_mul_f32_e32 v76, 0x3e0293ee, v76\n\t" \
  "v_mul_f32_e32 v77, 0x3e0293ee, v77\n\t" \
  "s_waitcnt lgkmcnt(6)\n\t" \
  "v_mfma_f32_32x32x16_bf16 v[96:111], a[236:239], a[40:43], v[96:111]\n\t" \
  "v_mul_f32_e32 v78, 0x3e0293ee, v78\n\t" \
  "v_mul_f32_e32 v79, 0x3e0293ee, v79\n\t" \
  "v_cvt_pk_bf16_f32 v72, v72, v73\n\t" \
  "v_cvt_pk_bf16_f32 v73, v74, v75\n\t" \
  "v_cvt_pk_bf16_f32 v74, v76, v77\n\t" \
  "v_cvt_pk_bf16_f32 v75, v78, v79\n\t" \
  "v_permlane32_swap_b32_e32 v64, v66\n\t" \
  "v_permlane32_swap_b32_e32 v65, v67\n\t" \
  "v_permlane32_swap_b32_e32 v72, v74\n\t" \
  "v_mfma_f32_32x32x16_bf16 v[112:127], a[236:239], a[56:59], v[112:127]\n\t" \
  "ds_read_b128 a[236:239], v227 offset:8192\n\t" \
  "v_permlane32_swap_b32_e32 v73, v75\n\t" \
  "v_accvgpr_write_b32 a144, v64\n\t" \
  "v_accvgpr_write_b32 a145, v65\n\t" \
  "v_accvgpr_write_b32 a146, v66\n\t" \
  "v_accvgpr_write_b32 a147, v67\n\t" \
  "v_accvgpr_write_b32 a148, v72\n\t" \
  "v_accvgpr_write_b32 a149, v73\n\t" \
  "v_accvgpr_write_b32 a150, v74\n\t" \
  "v_accvgpr_write_b32 a151, v75\n\t" \
  "s_waitcnt lgkmcnt(4)\n\t" \
  "v_mfma_f32_32x32x16_bf16 v[96:111], a[252:255], a[44:47], v[96:111]\n\t" \
  "v_mul_f32_e32 v80, 0x3e0293ee, v80\n\t" \
  "v_mul_f32_e32 v81, 0x3e0293ee, v81\n\t" \
  "v_mul_f32_e32 v82, 0x3e0293ee, v82\n\t" \
  "v_mul_f32_e32 v83, 0x3e0293ee, v83\n\t" \
  "v_mul_f32_e32 v84, 0x3e0293ee, v84\n\t" \
  "v_mul_f32_e32 v85, 0x3e0293ee, v85\n\t" \
  "v_mul_f32_e32 v86, 0x3e0293ee, v86\n\t" \
  "v_mul_f32_e32 v87, 0x3e0293ee, v87\n\t" \
  "v_cvt_pk_bf16_f32 v80, v80, v81\n\t" \
  "v_mfma_f32_32x32x16_bf16 v[112:127], a[252:255], a[60:63], v[112:127]\n\t" \
  "ds_read_b128 a[252:255], v227 offset:8320\n\t" \
  "v_cvt_pk_bf16_f32 v81, v82, v83\n\t" \
  "v_cvt_pk_bf16_f32 v82, v84, v85\n\t" \
  "v_cvt_pk_bf16_f32 v83, v86, v87\n\t" \
  "v_mul_f32_e32 v88, 0x3e0293ee, v88\n\t" \
  "v_mul_f32_e32 v89, 0x3e0293ee, v89\n\t" \
  "v_mul_f32_e32 v90, 0x3e0293ee, v90\n\t" \
  "v_mul_f32_e32 v91, 0x3e0293ee, v91\n\t" \
  "v_mul_f32_e32 v92, 0x3e0293ee, v92\n\t" \
  "v_mul_f32_e32 v93, 0x3e0293ee, v93\n\t" \
  "s_add_u32 m0, %[wom0], 0\n\t" \
  "s_mov_b32 s44, 0\n\t" \
  "buffer_load_dwordx4 v134, %[srdp], s44 offen lds\n\t" \
  "s_add_u32 m0, %[wom0], 1024\n\t" \
  "s_mov_b32 s44, 8192\n\t" \
  "buffer_load_dwordx4 v134, %[srdp], s44 offen lds\n\t" \
  "s_add_u32 m0, %[wom0], 2048\n\t" \
  "s_mov_b32 s44, 16384\n\t" \
  "buffer_load_dwordx4 v134, %[srdp], s44 offen lds\n\t" \
  "s_add_u32 m0, %[wom0], 3072\n\t" \
  "s_mov_b32 s44, 24576\n\t" \
  "buffer_load_dwordx4 v134, %[srdp], s44 offen lds\n\t" \
  "s_waitcnt lgkmcnt(0)\n\t" \
  "s_nop 11\n\t" \
  "" STAMP2_ASM "\n\t" \
  "s_mov_b32 s40, 0\n\t" \
  "s_mov_b32 s41, 8\n\t" \
  "s_xor_b32 %[m0v], %[m0v], 0x8000\n\t" \
  "s_waitcnt lgkmcnt(10)\n\t" \
  "v_mfma_f32_32x32x16_bf16 v[0:15], a[192:195], a[128:131], 0\n\t" \
  "v_mul_f32_e32 v94, 0x3e0293ee, v94\n\t" \
  "v_mul_f32_e32 v95, 0x3e0293ee, v95\n\t" \
  "v_cvt_pk_bf16_f32 v88, v88, v89\n\t" \
  "v_cvt_pk_bf16_f32 v89, v90, v91\n\t" \
  "v_cvt_pk_bf16_f32 v90, v92, v93\n\t" \
  "v_cvt_pk_bf16_f32 v91, v94, v95\n\t" \
  "v_permlane32_swap_b32_e32 v80, v82\n\t" \
  "v_permlane32_swap_b32_e32 v81, v83\n\t" \
  "v_permlane32_swap_b32_e32 v88, v90\n\t" \
  "v_mfma_f32_32x32x16_bf16 v[16:31], a[192:195], a[160:163], 0\n\t" \
  "v_permlane32_swap_b32_e32 v89, v91\n\t" \
  "v_accvgpr_write_b32 a176, v80\n\t" \
  "v_accvgpr_write_b32 a177, v81\n\t" \
  "v_accvgpr_write_b32 a178, v82\n\t" \
  "v_accvgpr_write_b32 a179, v83\n\t" \
  "v_accvgpr_write_b32 a180, v88\n\t" \
  "v_accvgpr_write_b32 a181, v89\n\t" \
  "v_accvgpr_write_b32 a182, v90\n\t" \
  "v_accvgpr_write_b32 a183, v91\n\t" \
  "v_mfma_f32_32x32x16_bf16 v[0:15], a[196:199], a[132:135], v[0:15]\n\t" \
  "v_mul_f32_e32 v96, 0x3e0293ee, v96\n\t" \
  "v_mul_f32_e32 v97, 0x3e0293ee, v97\n\t" \
  "v_mul_f32_e32 v98, 0x3e0293ee, v98\n\t" \
  "v_mul_f32_e32 v99, 0x3e0293ee, v99\n\t" \
  "v_mul_f32_e32 v100, 0x3e0293ee, v100\n\t" \
  "v_mul_f32_e32 v101, 0x3e0293ee, v101\n\t" \
  "v_mul_f32_e32 v102, 0x3e0293ee, v102\n\t" \
  "v_mul_f32_e32 v103, 0x3e0293ee, v103\n\t" \
  "v_cvt_pk_bf16_f32 v96, v96, v97\n\t" \
  "v_mfma_f32_32x32x16_bf16 v[16:31], a[196:199], a[164:167], v[16:31]\n\t" \
  "v_cvt_pk_bf16_f32 v97, v98, v99\n\t" \
  "v_cvt_pk_bf16_f32 v98, v100, v101\n\t" \
  "v_cvt_pk_bf16_f32 v99, v102, v103\n\t" \
  "v_mul_f32_e32 v104, 0x3e0293ee, v104\n\t" \
  "v_mul_f32_e32 v105, 0x3e0293ee, v105\n\t" \
  "v_mul_f32_e32 v106, 0x3e0293ee, v106\n\t" \
  "v_mul_f32_e32 v107, 0x3e0293ee, v107\n\t" \
  "v_mul_f32_e32 v108, 0x3e0293ee, v108\n\t" \
  "v_mul_f32_e32 v109, 0x3e0293ee, v109\n\t" \
  "v_mfma_f32_32x32x16_bf16 v[0:15], a[200:203], a[136:139], v[0:15]\n\t" \
  "v_mul_f32_e32 v110, 0x3e0293ee, v110\n\t" \
  "v_mul_f32_e32 v111, 0x3e0293ee, v111\n\t" \
  "v_cvt_pk_bf16_f32 v104, v104, v105\n\t" \
  "v_cvt_pk_bf16_f32 v105, v106, v107\n\t" \
  "v_cvt_pk_bf16_f32 v106, v108, v109\n\t" \
  "v_cvt_pk_bf16_f32 v107, v110, v111\n\t" \
  "v_permlane32_swap_b32_e32 v96, v98\n\t" \
  "v_permlane32_swap_b32_e32 v97, v99\n\t" \
  "v_permlane32_swap_b32_e32 v104, v106\n\t" \
  "v_mfma_f32_32x32x16_bf16 v[16:31], a[200:203], a[168:171], v[16:31]\n\t" \
  "v_permlane32_swap_b32_e32 v105, v107\n\t" \
  "v_accvgpr_write_b32 a152, v96\n\t" \
  "v_accvgpr_write_b32 a153, v97\n\t" \
  "v_accvgpr_write_b32 a154, v98\n\t" \
  "v_accvgpr_write_b32 a155, v99\n\t" \
  "v_accvgpr_write_b32 a156, v104\n\t" \
  "v_accvgpr_write_b32 a157, v105\n\t" \
  "v_accvgpr_write_b32 a158, v106\n\t" \
  "v_accvgpr_write_b32 a159, v107\n\t" \
  "v_mfma_f32_32x32x16_bf16 v[0:15], a[204:207], a[140:143], v[0:15]\n\t" \
  "v_mul_f32_e32 v112, 0x3e0293ee, v112\n\t" \
  "v_mul_f32_e32 v113, 0x3e0293ee, v113\n\t" \
  "v_mul_f32_e32 v114, 0x3e0293ee, v114\n\t" \
  "v_mul_f32_e32 v115, 0x3e0293ee, v115\n\t" \
  "v_mul_f32_e32 v116, 0x3e0293ee, v116\n\t" \
  "v_mul_f32_e32 v117, 0x3e0293ee, v117\n\t" \
  "v_mul_f32_e32 v118, 0x3e0293ee, v118\n\t" \
  "v_mul_f32_e32 v119, 0x3e0293ee, v119\n\t" \
  "v_cvt_pk_bf16_f32 v112, v112, v113\n\t" \
  "v_mfma_f32_32x32x16_bf16 v[16:31], a[204:207], a[172:175], v[16:31]\n\t" \
  "v_cvt_pk_bf16_f32 v113, v114, v115\n\t" \
  "v_cvt_pk_bf16_f32 v114, v116, v117\n\t" \
  "v_cvt_pk_bf16_f32 v115, v118, v119\n\t" \
  "v_mul_f32_e32 v120, 0x3e0293ee, v120\n\t" \
  "v_mul_f32_e32 v121, 0x3e0293ee, v121\n\t" \
  "v_mul_f32_e32 v122, 0x3e0293ee, v122\n\t" \
  "v_mul_f32_e32 v123, 0x3e0293ee, v123\n\t" \
  "v_mul_f32_e32 v124, 0x3e0293ee, v124\n\t" \
  "v_mul_f32_e32 v125, 0x3e0293ee, v125\n\t" \
  "v_mfma_f32_32x32x16_bf16 v[0:15], a[208:211], a[144:147], v[0:15]\n\t" \
  "v_mul_f32_e32 v126, 0x3e0293ee, v126\n\t" \
  "v_mul_f32_e32 v127, 0x3e0293ee, v127\n\t" \
  "v_cvt_pk_bf16_f32 v120, v120, v121\n\t" \
  "v_cvt_pk_bf16_f32 v121, v122, v123\n\t" \
  "v_cvt_pk_bf16_f32 v122, v124, v125\n\t" \
  "v_cvt_pk_bf16_f32 v123, v126, v127\n\t" \
  "v_permlane32_swap_b32_e32 v112, v114\n\t" \
  "v_permlane32_swap_b32_e32 v113, v115\n\t" \
  "v_permlane32_swap_b32_e32 v120, v122\n\t" \
  "v_mfma_f32_32x32x16_bf16 v[16:31], a[208:211], a[176:179], v[16:31]\n\t" \
  "v_permlane32_swap_b32_e32 v121, v123\n\t" \
  "v_accvgpr_write_b32 a184, v112\n\t" \
  "v_accvgpr_write_b32 a185, v113\n\t" \
  "v_accvgpr_write_b32 a186, v114\n\t" \
  "v_accvgpr_write_b32 a187, v115\n\t" \
  "v_accvgpr_write_b32 a188, v120\n\t" \
  "v_accvgpr_write_b32 a189, v121\n\t" \
  "v_accvgpr_write_b32 a190, v122\n\t" \
  "v_accvgpr_write_b32 a191, v123\n\t" \
  "v_mfma_f32_32x32x16_bf16 v[0:15], a[212:215], a[148:151], v[0:15]\n\t" \
  "v_mfma_f32_32x32x16_bf16 v[16:31], a[212:215], a[180:183], v[16:31]\n\t" \
  "v_mfma_f32_32x32x16_bf16 v[0:15], a[216:219], a[152:155], v[0:15]\n\t" \
  "v_mfma_f32_32x32x16_bf16 v[16:31], a[216:219], a[184:187], v[16:31]\n\t" \
  "v_mfma_f32_32x32x16_bf16 v[0:15], a[220:223], a[156:159], v[0:15]\n\t" \
  "v_mfma_f32_32x32x16_bf16 v[16:31], a[220:223], a[188:191], v[16:31]\n\t" \
  "s_add_u32 m0, %[m0k], 0\n\t" \
  "s_waitcnt vmcnt(16) lgkmcnt(0)\n\t" \
  "s_barrier\n\t" \
  "v_mfma_f32_32x32x16_bf16 v[32:47], a[224:227], a[128:131], 0\n\t" \
  "buffer_load_dwordx4 v229, %[srdk], %[koff] offen nt lds\n\t" \
  "v_mfma_f32_32x32x16_bf16 v[48:63], a[224:227], a[160:163], 0\n\t" \
  "v_mfma_f32_32x32x16_bf16 v[32:47], a[228:231], a[132:135], v[32:47]\n\t" \
  "buffer_load_dwordx4 v230, %[srdk], %[koff] offen offset:1024 nt lds\n\t" \
  "v_mfma_f32_32x32x16_bf16 v[48:63], a[228:231], a[164:167], v[48:63]\n\t" \
  "s_nop 1\n\t" \
  "v_max_f32_e32 v241, v0, v1\n\t" \
  "v_max_f32_e32 v242, v16, v17\n\t" \
  "v_mfma_f32_32x32x16_bf16 v[32:47], a[232:235], a[136:139], v[32:47]\n\t" \
  "buffer_load_dwordx4 v229, %[srdk], %[koff] offen offset:2048 nt lds\n\t" \
  "v_mov_b32_e32 v160, 0\n\t" \
  "v_mov_b32_e32 v161, 0\n\t" \
  "v_mov_b32_e32 v162, 0\n\t" \
  "v_mov_b32_e32 v163, 0\n\t" \
  "v_mfma_f32_32x32x16_bf16 v[48:63], a[232:235], a[168:171], v[48:63]\n\t" \
  "v_max3_f32 v241, v241, v2, v3\n\t" \
  "v_max3_f32 v242, v242, v18, v19\n\t" \
  "v_mfma_f32_32x32x16_bf16 v[32:47], a[236:239], a[140:143], v[32:47]\n\t" \
  "buffer_load_dwordx4 v230, %[srdk], %[koff] offen offset:3072 nt lds\n\t" \
  "v_mfma_f32_32x32x16_bf16 v[48:63], a[236:239], a[172:175], v[48:63]\n\t" \
  "v_max3_f32 v241, v241, v4, v5\n\t" \
  "v_max3_f32 v242, v242, v20, v21\n\t" \
  "s_add_u32 m0, %[m0v], 16384\n\t" \
  "v_mfma_f32_32x32x16_bf16 v[32:47], a[240:243], a[144:147], v[32:47]\n\t" \
  "buffer_load_dwordx4 v231, %[srdv], %[voff] offen nt lds\n\t" \
  "v_mfma_f32_32x32x16_bf16 v[48:63], a[240:243], a[176:179], v[48:63]\n\t" \
  "v_max3_f32 v241, v241, v6, v7\n\t" \
  "v_max3_f32 v242, v242, v22, v23\n\t" \
  "v_mfma_f32_32x32x16_bf16 v[32:47], a[244:247], a[148:151], v[32:47]\n\t" \
  "buffer_load_dwordx4 v231, %[srdv], %[voff] offen offset:2048 nt lds\n\t" \
  "v_mfma_f32_32x32x16_bf16 v[48:63], a[244:247], a[180:183], v[48:63]\n\t" \
  "v_max3_f32 v241, v241, v8, v9\n\t" \
  "v_max3_f32 v242, v242, v24, v25\n\t" \
  "s_add_u32 m0, %[m0v], 20480\n\t" \
  "v_mfma_f32_32x32x16_bf16 v[32:47], a[248:251], a[152:155], v[32:47]\n\t" \
  "buffer_load_dwordx4 v232, %[srdv], %[voff] offen nt lds\n\t" \
  "v_mfma_f32_32x32x16_bf16 v[48:63], a[248:251], a[184:187], v[48:63]\n\t" \
  "v_max3_f32 v241, v241, v10, v11\n\t" \
  "v_max3_f32 v242, v242, v26, v27\n\t" \
  "v_mfma_f32_32x32x16_bf16 v[32:47], a[252:255], a[156:159], v[32:47]\n\t" \
  "buffer_load_dwordx4 v232, %[srdv], %[voff] offen offset:2048 nt lds\n\t" \
  "v_mfma_f32_32x32x16_bf16 v[48:63], a[252:255], a[188:191], v[48:63]\n\t" \
  "v_max3_f32 v241, v241, v12, v13\n\t" \
  "v_max3_f32 v242, v242, v28, v29\n\t" \
  "v_mfma_f32_32x32x16_bf16 a[0:15], v[160:163], v[160:163], 0\n\t" \
  "v_max3_f32 v241, v241, v14, v15\n\t" \
  "s_add_u32 %[koff], %[koff], 16384\n\t" \
  "v_mfma_f32_32x32x16_bf16 a[16:31], v[160:163], v[160:163], 0\n\t" \
  "v_max3_f32 v242, v242, v30, v31\n\t" \
  "s_add_u32 %[voff], %[voff], 16384\n\t" \
  "v_mfma_f32_32x32x16_bf16 a[32:47], v[160:163], v[160:163], 0\n\t" \
  "s_xor_b32 %[m0v], %[m0v], 0x8000\n\t" \
  "v_mfma_f32_32x32x16_bf16 a[48:63], v[160:163], v[160:163], 0\n\t" \
  "v_max3_f32 v241, v241, v32, v33\n\t" \
  "v_max3_f32 v242, v242, v48, v49\n\t" \
  "v_mfma_f32_32x32x16_bf16 a[64:79], v[160:163], v[160:163], 0\n\t" \
  "v_max3_f32 v241, v241, v34, v35\n\t" \
  "v_max3_f32 v242, v242, v50, v51\n\t" \
  "v_mfma_f32_32x32x16_bf16 a[80:95], v[160:163], v[160:163], 0\n\t" \
  "v_max3_f32 v241, v241, v36, v37\n\t" \
  "v_max3_f32 v242, v242, v52, v53\n\t" \
  "v_mfma_f32_32x32x16_bf16 a[96:111], v[160:163], v[160:163], 0\n\t" \
  "v_max3_f32 v241, v241, v38, v39\n\t" \
  "v_max3_f32 v242, v242, v54, v55\n\t" \
  "v_mfma_f32_32x32x16_bf16 a[112:127], v[160:163], v[160:163], 0\n\t" \
  "v_max3_f32 v241, v241, v40, v41\n\t" \
  "v_max3_f32 v242, v242, v56, v57\n\t" \
  "v_max3_f32 v241, v241, v42, v43\n\t" \
  "v_max3_f32 v242, v242, v58, v59\n\t" \
  "v_max3_f32 v241, v241, v44, v45\n\t" \
  "v_max3_f32 v242, v242, v60, v61\n\t" \
  "v_max3_f32 v241, v241, v46, v47\n\t" \
  "v_max3_f32 v242, v242, v62, v63\n\t" \
  "v_mov_b32_e32 v243, v241\n\t" \
  "v_mov_b32_e32 v244, v242\n\t" \
  "s_nop 0\n\t" \
  "v_permlane32_swap_b32_e32 v241, v243\n\t" \
  "v_permlane32_swap_b32_e32 v242, v244\n\t" \
  "v_max_f32_e32 v245, v241, v243\n\t" \
  "v_max_f32_e32 v246, v242, v244\n\t" \
  "v_mov_b32_e32 v237, v245\n\t" \
  "v_mov_b32_e32 v239, 0\n\t" \
  "v_sub_f32_e32 v0, v0, v245\n\t" \
  "v_sub_f32_e32 v1, v1, v245\n\t" \
  "v_sub_f32_e32 v2, v2, v245\n\t" \
  "v_sub_f32_e32 v3, v3, v245\n\t" \
  "v_sub_f32_e32 v4, v4, v245\n\t" \
  "v_sub_f32_e32 v5, v5, v245\n\t" \
  "v_sub_f32_e32 v6, v6, v245\n\t" \
  "v_sub_f32_e32 v7, v7, v245\n\t" \
  "v_sub_f32_e32 v8, v8, v245\n\t" \
  "v_sub_f32_e32 v9, v9, v245\n\t" \
  "v_sub_f32_e32 v10, v10, v245\n\t" \
  "v_sub_f32_e32 v11, v11, v245\n\t" \
  "v_sub_f32_e32 v12, v12, v245\n\t" \
  "v_sub_f32_e32 v13, v13, v245\n\t" \
  "v_sub_f32_e32 v14, v14, v245\n\t" \
  "v_sub_f32_e32 v15, v15, v245\n\t" \
  "v_sub_f32_e32 v32, v32, v245\n\t" \
  "v_sub_f32_e32 v33, v33, v245\n\t" \
  "v_sub_f32_e32 v34, v34, v245\n\t" \
  "v_sub_f32_e32 v35, v35, v245\n\t" \
  "v_sub_f32_e32 v36, v36, v245\n\t" \
  "v_sub_f32_e32 v37, v37, v245\n\t" \
  "v_sub_f32_e32 v38, v38, v245\n\t" \
  "v_sub_f32_e32 v39, v39, v245\n\t" \
  "v_sub_f32_e32 v40, v40, v245\n\t" \
  "v_sub_f32_e32 v41, v41, v245\n\t" \
  "v_sub_f32_e32 v42, v42, v245\n\t" \
  "v_sub_f32_e32 v43, v43, v245\n\t" \
  "v_sub_f32_e32 v44, v44, v245\n\t" \
  "v_sub_f32_e32 v45, v45, v245\n\t" \
  "v_sub_f32_e32 v46, v46, v245\n\t" \
  "v_sub_f32_e32 v47, v47, v245\n\t" \
  "v_sub_f32_e32 v128, 0, v245\n\t" \
  "v_mov_b32_e32 v129, v128\n\t" \
  "v_mov_b32_e32 v130, v128\n\t" \
  "v_mov_b32_e32 v131, v128\n\t" \
  "v_mov_b32_e32 v132, v128\n\t" \
  "v_mov_b32_e32 v133, v128\n\t" \
  "v_mov_b32_e32 v134, v128\n\t" \
  "v_mov_b32_e32 v135, v128\n\t" \
  "v_mov_b32_e32 v136, v128\n\t" \
  "v_mov_b32_e32 v137, v128\n\t" \
  "v_mov_b32_e32 v138, v128\n\t" \
  "v_mov_b32_e32 v139, v128\n\t" \
  "v_mov_b32_e32 v140, v128\n\t" \
  "v_mov_b32_e32 v141, v128\n\t" \
  "v_mov_b32_e32 v142, v128\n\t" \
  "v_mov_b32_e32 v143, v128\n\t" \
  "v_mov_b32_e32 v238, v246\n\t" \
  "v_mov_b32_e32 v240, 0\n\t" \
  "v_sub_f32_e32 v16, v16, v246\n\t" \
  "v_sub_f32_e32 v17, v17, v246\n\t" \
  "v_sub_f32_e32 v18, v18, v246\n\t" \
  "v_sub_f32_e32 v19, v19, v246\n\t" \
  "v_sub_f32_e32 v20, v20, v246\n\t" \
  "v_sub_f32_e32 v21, v21, v246\n\t" \
  "v_sub_f32_e32 v22, v22, v246\n\t" \
  "v_sub_f32_e32 v23, v23, v246\n\t" \
  "v_sub_f32_e32 v24, v24, v246\n\t" \
  "v_sub_f32_e32 v25, v25, v246\n\t" \
  "v_sub_f32_e32 v26, v26, v246\n\t" \
  "v_sub_f32_e32 v27, v27, v246\n\t" \
  "v_sub_f32_e32 v28, v28, v246\n\t" \
  "v_sub_f32_e32 v29, v29, v246\n\t" \
  "v_sub_f32_e32 v30, v30, v246\n\t" \
  "v_sub_f32_e32 v31, v31, v246\n\t" \
  "v_sub_f32_e32 v48, v48, v246\n\t" \
  "v_sub_f32_e32 v49, v49, v246\n\t" \
  "v_sub_f32_e32 v50, v50, v246\n\t" \
  "v_sub_f32_e32 v51, v51, v246\n\t" \
  "v_sub_f32_e32 v52, v52, v246\n\t" \
  "v_sub_f32_e32 v53, v53, v246\n\t" \
  "v_sub_f32_e32 v54, v54, v246\n\t" \
  "v_sub_f32_e32 v55, v55, v246\n\t" \
  "v_sub_f32_e32 v56, v56, v246\n\t" \
  "v_sub_f32_e32 v57, v57, v246\n\t" \
  "v_sub_f32_e32 v58, v58, v246\n\t" \
  "v_sub_f32_e32 v59, v59, v246\n\t" \
  "v_sub_f32_e32 v60, v60, v246\n\t" \
  "v_sub_f32_e32 v61, v61, v246\n\t" \
  "v_sub_f32_e32 v62, v62, v246\n\t" \
  "v_sub_f32_e32 v63, v63, v246\n\t" \
  "v_sub_f32_e32 v144, 0, v246\n\t" \
  "v_mov_b32_e32 v145, v144\n\t" \
  "v_mov_b32_e32 v146, v144\n\t" \
  "v_mov_b32_e32 v147, v144\n\t" \
  "v_mov_b32_e32 v148, v144\n\t" \
  "v_mov_b32_e32 v149, v144\n\t" \
  "v_mov_b32_e32 v150, v144\n\t" \
  "v_mov_b32_e32 v151, v144\n\t" \
  "v_mov_b32_e32 v152, v144\n\t" \
  "v_mov_b32_e32 v153, v144\n\t" \
  "v_mov_b32_e32 v154, v144\n\t" \
  "v_mov_b32_e32 v155, v144\n\t" \
  "v_mov_b32_e32 v156, v144\n\t" \
  "v_mov_b32_e32 v157, v144\n\t" \
  "v_mov_b32_e32 v158, v144\n\t" \
  "v_mov_b32_e32 v159, v144\n\t" \
  "ds_read_b64_tr_b16 v[160:161], v228 offset:0\n\t" \
  "ds_read_b128 a[192:195], v224 offset:16384\n\t" \
  "v_exp_f32_e32 v0, v0\n\t" \
  "ds_read_b64_tr_b16 v[162:163], v228 offset:2048\n\t" \
  "ds_read_b128 a[196:199], v225 offset:16384\n\t" \
  "v_exp_f32_e32 v1, v1\n\t" \
  "v_mov_b32_e32 v233, v0\n\t" \
  "ds_read_b64_tr_b16 v[164:165], v228 offset:512\n\t" \
  "ds_read_b128 a[200:203], v226 offset:16384\n\t" \
  "v_exp_f32_e32 v2, v2\n\t" \
  "v_add_f32_e32 v233, v233, v1\n\t" \
  "v_cvt_pk_bf16_f32 v0, v0, v1\n\t" \
  "ds_read_b64_tr_b16 v[166:167], v228 offset:2560\n\t" \
  "ds_read_b128 a[204:207], v227 offset:16384\n\t" \
  "v_exp_f32_e32 v3, v3\n\t" \
  "v_add_f32_e32 v233, v233, v2\n\t" \
  "ds_read_b64_tr_b16 v[168:169], v228 offset:1024\n\t" \
  "ds_read_b128 a[208:211], v224 offset:16512\n\t" \
  "v_exp_f32_e32 v4, v4\n\t" \
  "v_add_f32_e32 v233, v233, v3\n\t" \
  "v_cvt_pk_bf16_f32 v1, v2, v3\n\t" \
  "ds_read_b64_tr_b16 v[170:171], v228 offset:3072\n\t" \
  "ds_read_b128 a[212:215], v225 offset:16512\n\t" \
  "v_exp_f32_e32 v5, v5\n\t" \
  "v_add_f32_e32 v233, v233, v4\n\t" \
  "ds_read_b64_tr_b16 v[172:173], v228 offset:1536\n\t" \
  "ds_read_b128 a[216:219], v226 offset:16512\n\t" \
  "v_exp_f32_e32 v6, v6\n\t" \
  "v_add_f32_e32 v233, v233, v5\n\t" \
  "v_cvt_pk_bf16_f32 v2, v4, v5\n\t" \
  "ds_read_b64_tr_b16 v[174:175], v228 offset:3584\n\t" \
  "ds_read_b128 a[220:223], v227 offset:16512\n\t" \
  "v_exp_f32_e32 v7, v7\n\t" \
  "v_add_f32_e32 v233, v233, v6\n\t" \
  "ds_read_b64_tr_b16 v[176:177], v228 offset:4096\n\t" \
  "ds_read_b128 a[224:227], v224 offset:24576\n\t" \
  "v_exp_f32_e32 v16, v16\n\t" \
  "v_add_f32_e32 v233, v233, v7\n\t" \
  "v_cvt_pk_bf16_f32 v3, v6, v7\n\t" \
  "ds_read_b64_tr_b16 v[178:179], v228 offset:6144\n\t" \
  "ds_read_b128 a[228:231], v225 offset:24576\n\t" \
  "v_exp_f32_e32 v17, v17\n\t" \
  "v_mov_b32_e32 v234, v16\n\t" \
  "ds_read_b64_tr_b16 v[180:181], v228 offset:4608\n\t" \
  "ds_read_b128 a[232:235], v226 offset:24576\n\t" \
  "v_exp_f32_e32 v18, v18\n\t" \
  "v_add_f32_e32 v234, v234, v17\n\t" \
  "v_cvt_pk_bf16_f32 v16, v16, v17\n\t" \
  "ds_read_b64_tr_b16 v[182:183], v228 offset:6656\n\t" \
  "ds_read_b128 a[236:239], v227 offset:24576\n\t" \
  "v_exp_f32_e32 v19, v19\n\t" \
  "v_add_f32_e32 v234, v234, v18\n\t" \
  "ds_read_b64_tr_b16 v[184:185], v228 offset:5120\n\t" \
  "ds_read_b128 a[240:243], v224 offset:24704\n\t" \
  "v_exp_f32_e32 v20, v20\n\t" \
  "v_add_f32_e32 v234, v234, v19\n\t" \
  "v_cvt_pk_bf16_f32 v17, v18, v19\n\t" \
  "ds_read_b64_tr_b16 v[186:187], v228 offset:7168\n\t" \
  "ds_read_b128 a[244:247], v225 offset:24704\n\t" \
  "v_exp_f32_e32 v21, v21\n\t" \
  "v_add_f32_e32 v234, v234, v20\n\t" \
  "ds_read_b64_tr_b16 v[188:189], v228 offset:5632\n\t" \
  "ds_read_b128 a[248:251], v226 offset:24704\n\t" \
  "v_exp_f32_e32 v22, v22\n\t" \
  "v_add_f32_e32 v234, v234, v21\n\t" \
  "v_cvt_pk_bf16_f32 v18, v20, v21\n\t" \
  "ds_read_b64_tr_b16 v[190:191], v228 offset:7680\n\t" \
  "ds_read_b128 a[252:255], v227 offset:24704\n\t" \
  "v_exp_f32_e32 v23, v23\n\t" \
  "v_add_f32_e32 v234, v234, v22\n\t" \
  "ds_read_b64_tr_b16 v[192:193], v228 offset:8192\n\t" \
  "v_exp_f32_e32 v8, v8\n\t" \
  "v_add_f32_e32 v234, v234, v23\n\t" \
  "v_cvt_pk_bf16_f32 v19, v22, v23\n\t" \
  "ds_read_b64_tr_b16 v[194:195], v228 offset:10240\n\t" \
  "v_exp_f32_e32 v9, v9\n\t" \
  "v_add_f32_e32 v233, v233, v8\n\t" \
  "LLOOP_%=:\n\t" \
  "s_waitcnt lgkmcnt(10)\n\t" \
  "v_mfma_f32_32x32x16_bf16 v[64:79], a[192:195], a[128:131], v[128:143]\n\t" \
  "ds_read_b64_tr_b16 v[196:197], v228 offset:8704\n\t" \
  "v_exp_f32_e32 v10, v10\n\t" \
  "v_add_f32_e32 v233, v233, v9\n\t" \
  "v_cvt_pk_bf16_f32 v8, v8, v9\n\t" \
  "v_mfma_f32_32x32x16_bf16 v[80:95], a[192:195], a[160:163], v[144:159]\n\t" \
  "ds_read_b64_tr_b16 v[198:199], v228 offset:10752\n\t" \
  "v_exp_f32_e32 v11, v11\n\t" \
  "v_add_f32_e32 v233, v233, v10\n\t" \
  "v_mfma_f32_32x32x16_bf16 v[64:79], a[196:199], a[132:135], v[64:79]\n\t" \
  "ds_read_b64_tr_b16 v[200:201], v228 offset:9216\n\t" \
  "v_exp_f32_e32 v12, v12\n\t" \
  "v_add_f32_e32 v233, v233, v11\n\t" \
  "v_cvt_pk_bf16_f32 v9, v10, v11\n\t" \
  "v_mfma_f32_32x32x16_bf16 v[80:95], a[196:199], a[164:167], v[80:95]\n\t" \
  "ds_read_b64_tr_b16 v[202:203], v228 offset:11264\n\t" \
  "v_exp_f32_e32 v13, v13\n\t" \
  "v_add_f32_e32 v233, v233, v12\n\t" \
  "v_mfma_f32_32x32x16_bf16 v[64:79], a[200:203], a[136:139], v[64:79]\n\t" \
  "ds_read_b64_tr_b16 v[204:205], v228 offset:9728\n\t" \
  "v_exp_f32_e32 v14, v14\n\t" \
  "v_add_f32_e32 v233, v233, v13\n\t" \
  "v_cvt_pk_bf16_f32 v10, v12, v13\n\t" \
  "v_mfma_f32_32x32x16_bf16 v[80:95], a[200:203], a[168:171], v[80:95]\n\t" \
  "ds_read_b64_tr_b16 v[206:207], v228 offset:11776\n\t" \
  "v_exp_f32_e32 v15, v15\n\t" \
  "v_add_f32_e32 v233, v233, v14\n\t" \
  "v_mfma_f32_32x32x16_bf16 v[64:79], a[204:207], a[140:143], v[64:79]\n\t" \
  "ds_read_b64_tr_b16 v[208:209], v228 offset:12288\n\t" \
  "v_exp_f32_e32 v24, v24\n\t" \
  "v_add_f32_e32 v233, v233, v15\n\t" \
  "v_cvt_pk_bf16_f32 v11, v14, v15\n\t" \
  "v_mfma_f32_32x32x16_bf16 v[80:95], a[204:207], a[172:175], v[80:95]\n\t" \
  "ds_read_b64_tr_b16 v[210:211], v228 offset:14336\n\t" \
  "v_exp_f32_e32 v25, v25\n\t" \
  "v_add_f32_e32 v234, v234, v24\n\t" \
  "v_mfma_f32_32x32x16_bf16 v[64:79], a[208:211], a[144:147], v[64:79]\n\t" \
  "ds_read_b64_tr_b16 v[212:213], v228 offset:12800\n\t" \
  "v_exp_f32_e32 v26, v26\n\t" \
  "v_add_f32_e32 v234, v234, v25\n\t" \
  "v_cvt_pk_bf16_f32 v24, v24, v25\n\t" \
  "v_mfma_f32_32x32x16_bf16 v[80:95], a[208:211], a[176:179], v[80:95]\n\t" \
  "ds_read_b64_tr_b16 v[214:215], v228 offset:14848\n\t" \
  "v_exp_f32_e32 v27, v27\n\t" \
  "v_add_f32_e32 v234, v234, v26\n\t" \
  "v_mfma_f32_32x32x16_bf16 v[64:79], a[212:215], a[148:151], v[64:79]\n\t" \
  "ds_read_b64_tr_b16 v[216:217], v228 offset:13312\n\t" \
  "v_exp_f32_e32 v28, v28\n\t" \
  "v_add_f32_e32 v234, v234, v27\n\t" \
  "v_cvt_pk_bf16_f32 v25, v26, v27\n\t" \
  "v_mfma_f32_32x32x16_bf16 v[80:95], a[212:215], a[180:183], v[80:95]\n\t" \
  "ds_read_b64_tr_b16 v[218:219], v228 offset:15360\n\t" \
  "v_exp_f32_e32 v29, v29\n\t" \
  "v_add_f32_e32 v234, v234, v28\n\t" \
  "v_mfma_f32_32x32x16_bf16 v[64:79], a[216:219], a[152:155], v[64:79]\n\t" \
  "ds_read_b64_tr_b16 v[220:221], v228 offset:13824\n\t" \
  "v_exp_f32_e32 v30, v30\n\t" \
  "v_add_f32_e32 v234, v234, v29\n\t" \
  "v_cvt_pk_bf16_f32 v26, v28, v29\n\t" \
  "v_mfma_f32_32x32x16_bf16 v[80:95], a[216:219], a[184:187], v[80:95]\n\t" \
  "ds_read_b64_tr_b16 v[222:223], v228 offset:15872\n\t" \
  "v_exp_f32_e32 v31, v31\n\t" \
  "v_add_f32_e32 v234, v234, v30\n\t" \
  "v_mfma_f32_32x32x16_bf16 v[64:79], a[220:223], a[156:159], v[64:79]\n\t" \
  "v_exp_f32_e32 v32, v32\n\t" \
  "v_add_f32_e32 v234, v234, v31\n\t" \
  "v_cvt_pk_bf16_f32 v27, v30, v31\n\t" \
  "v_xor_b32_e32 v224, 0x8000, v224\n\t" \
  "v_xor_b32_e32 v225, 0x8000, v225\n\t" \
  "v_mfma_f32_32x32x16_bf16 v[80:95], a[220:223], a[188:191], v[80:95]\n\t" \
  "v_exp_f32_e32 v33, v33\n\t" \
  "v_add_f32_e32 v233, v233, v32\n\t" \
  "v_xor_b32_e32 v226, 0x8000, v226\n\t" \
  "v_xor_b32_e32 v227, 0x8000, v227\n\t" \
  "s_add_u32 m0, %[m0k], 16384\n\t" \
  "s_waitcnt vmcnt(16) lgkmcnt(0)\n\t" \
  "s_barrier\n\t" \
  "v_mfma_f32_32x32x16_bf16 v[96:111], a[224:227], a[128:131], v[128:143]\n\t" \
  "buffer_load_dwordx4 v229, %[srdk], %[koff] offen nt lds\n\t" \
  "v_exp_f32_e32 v34, v34\n\t" \
  "v_add_f32_e32 v233, v233, v33\n\t" \
  "v_cvt_pk_bf16_f32 v32, v32, v33\n\t" \
  "v_mfma_f32_32x32x16_bf16 v[112:127], a[224:227], a[160:163], v[144:159]\n\t" \
  "v_exp_f32_e32 v35, v35\n\t" \
  "v_add_f32_e32 v233, v233, v34\n\t" \
  "v_mfma_f32_32x32x16_bf16 v[96:111], a[228:231], a[132:135], v[96:111]\n\t" \
  "buffer_load_dwordx4 v230, %[srdk], %[koff] offen offset:1024 nt lds\n\t" \
  "v_exp_f32_e32 v36, v36\n\t" \
  "v_add_f32_e32 v233, v233, v35\n\t" \
  "v_cvt_pk_bf16_f32 v33, v34, v35\n\t" \
  "v_mfma_f32_32x32x16_bf16 v[112:127], a[228:231], a[164:167], v[112:127]\n\t" \
  "v_exp_f32_e32 v37, v37\n\t" \
  "v_add_f32_e32 v233, v233, v36\n\t" \
  "v_max_f32_e32 v241, v64, v65\n\t" \
  "v_max_f32_e32 v242, v80, v81\n\t" \
  "v_mfma_f32_32x32x16_bf16 v[96:111], a[232:235], a[136:139], v[96:111]\n\t" \
  "buffer_load_dwordx4 v229, %[srdk], %[koff] offen offset:2048 nt lds\n\t" \
  "v_exp_f32_e32 v38, v38\n\t" \
  "v_add_f32_e32 v233, v233, v37\n\t" \
  "v_cvt_pk_bf16_f32 v34, v36, v37\n\t" \
  "v_mfma_f32_32x32x16_bf16 v[112:127], a[232:235], a[168:171], v[112:127]\n\t" \
  "v_exp_f32_e32 v39, v39\n\t" \
  "v_add_f32_e32 v233, v233, v38\n\t" \
  "v_max3_f32 v241, v241, v66, v67\n\t" \
  "v_max3_f32 v242, v242, v82, v83\n\t" \
  "v_mfma_f32_32x32x16_bf16 v[96:111], a[236:239], a[140:143], v[96:111]\n\t" \
  "buffer_load_dwordx4 v230, %[srdk], %[koff] offen offset:3072 nt lds\n\t" \
  "v_exp_f32_e32 v48, v48\n\t" \
  "v_add_f32_e32 v233, v233, v39\n\t" \
  "v_cvt_pk_bf16_f32 v35, v38, v39\n\t" \
  "v_mfma_f32_32x32x16_bf16 v[112:127], a[236:239], a[172:175], v[112:127]\n\t" \
  "v_exp_f32_e32 v49, v49\n\t" \
  "v_add_f32_e32 v234, v234, v48\n\t" \
  "v_max3_f32 v241, v241, v68, v69\n\t" \
  "v_max3_f32 v242, v242, v84, v85\n\t" \
  "s_add_u32 m0, %[m0v], 0\n\t" \
  "v_mfma_f32_32x32x16_bf16 v[96:111], a[240:243], a[144:147], v[96:111]\n\t" \
  "buffer_load_dwordx4 v231, %[srdv], %[voff] offen nt lds\n\t" \
  "v_exp_f32_e32 v50, v50\n\t" \
  "v_add_f32_e32 v234, v234, v49\n\t" \
  "v_cvt_pk_bf16_f32 v48, v48, v49\n\t" \
  "v_mfma_f32_32x32x16_bf16 v[112:127], a[240:243], a[176:179], v[112:127]\n\t" \
  "v_exp_f32_e32 v51, v51\n\t" \
  "v_add_f32_e32 v234, v234, v50\n\t" \
  "v_max3_f32 v241, v241, v70, v71\n\t" \
  "v_max3_f32 v242, v242, v86, v87\n\t" \
  "v_mfma_f32_32x32x16_bf16 v[96:111], a[244:247], a[148:151], v[96:111]\n\t" \
  "buffer_load_dwordx4 v231, %[srdv], %[voff] offen offset:2048 nt lds\n\t" \
  "v_exp_f32_e32 v52, v52\n\t" \
  "v_add_f32_e32 v234, v234, v51\n\t" \
  "v_cvt_pk_bf16_f32 v49, v50, v51\n\t" \
  "v_mfma_f32_32x32x16_bf16 v[112:127], a[244:247], a[180:183], v[112:127]\n\t" \
  "v_exp_f32_e32 v53, v53\n\t" \
  "v_add_f32_e32 v234, v234, v52\n\t" \
  "v_max3_f32 v241, v241, v72, v73\n\t" \
  "v_max3_f32 v242, v242, v88, v89\n\t" \
  "s_add_u32 m0, %[m0v], 4096\n\t" \
  "v_mfma_f32_32x32x16_bf16 v[96:111], a[248:251], a[152:155], v[96:111]\n\t" \
  "buffer_load_dwordx4 v232, %[srdv], %[voff] offen nt lds\n\t" \
  "v_exp_f32_e32 v54, v54\n\t" \
  "v_add_f32_e32 v234, v234, v53\n\t" \
  "v_cvt_pk_bf16_f32 v50, v52, v53\n\t" \
  "v_mfma_f32_32x32x16_bf16 v[112:127], a[248:251], a[184:187], v[112:127]\n\t" \
  "v_exp_f32_e32 v55, v55\n\t" \
  "v_add_f32_e32 v234, v234, v54\n\t" \
  "v_max3_f32 v241, v241, v74, v75\n\t" \
  "v_max3_f32 v242, v242, v90, v91\n\t" \
  "v_mfma_f32_32x32x16_bf16 v[96:111], a[252:255], a[156:159], v[96:111]\n\t" \
  "buffer_load_dwordx4 v232, %[srdv], %[voff] offen offset:2048 nt lds\n\t" \
  "v_exp_f32_e32 v40, v40\n\t" \
  "v_add_f32_e32 v234, v234, v55\n\t" \
  "v_cvt_pk_bf16_f32 v51, v54, v55\n\t" \
  "v_mfma_f32_32x32x16_bf16 v[112:127], a[252:255], a[188:191], v[112:127]\n\t" \
  "v_exp_f32_e32 v41, v41\n\t" \
  "v_add_f32_e32 v233, v233, v40\n\t" \
  "v_max3_f32 v241, v241, v76, v77\n\t" \
  "v_max3_f32 v242, v242, v92, v93\n\t" \
  "v_mfma_f32_32x32x16_bf16 a[0:15], v[160:163], v[0:3], a[0:15]\n\t" \
  "v_exp_f32_e32 v42, v42\n\t" \
  "v_add_f32_e32 v233, v233, v41\n\t" \
  "v_cvt_pk_bf16_f32 v40, v40, v41\n\t" \
  "v_max3_f32 v241, v241, v78, v79\n\t" \
  "s_add_u32 %[koff], %[koff], 16384\n\t" \
  "v_mfma_f32_32x32x16_bf16 a[16:31], v[160:163], v[16:19], a[16:31]\n\t" \
  "v_exp_f32_e32 v43, v43\n\t" \
  "v_add_f32_e32 v233, v233, v42\n\t" \
  "v_max3_f32 v242, v242, v94, v95\n\t" \
  "s_add_u32 %[voff], %[voff], 16384\n\t" \
  "v_mfma_f32_32x32x16_bf16 a[32:47], v[164:167], v[0:3], a[32:47]\n\t" \
  "v_exp_f32_e32 v44, v44\n\t" \
  "v_add_f32_e32 v233, v233, v43\n\t" \
  "v_cvt_pk_bf16_f32 v41, v42, v43\n\t" \
  "v_mfma_f32_32x32x16_bf16 a[48:63], v[164:167], v[16:19], a[48:63]\n\t" \
  "v_exp_f32_e32 v45, v45\n\t" \
  "v_add_f32_e32 v233, v233, v44\n\t" \
  "v_max3_f32 v241, v241, v96, v97\n\t" \
  "v_max3_f32 v242, v242, v112, v113\n\t" \
  "v_mfma_f32_32x32x16_bf16 a[64:79], v[168:171], v[0:3], a[64:79]\n\t" \
  "v_exp_f32_e32 v46, v46\n\t" \
  "v_add_f32_e32 v233, v233, v45\n\t" \
  "v_cvt_pk_bf16_f32 v42, v44, v45\n\t" \
  "v_max3_f32 v241, v241, v98, v99\n\t" \
  "v_max3_f32 v242, v242, v114, v115\n\t" \
  "v_mfma_f32_32x32x16_bf16 a[80:95], v[168:171], v[16:19], a[80:95]\n\t" \
  "v_exp_f32_e32 v47, v47\n\t" \
  "v_add_f32_e32 v233, v233, v46\n\t" \
  "v_max3_f32 v241, v241, v100, v101\n\t" \
  "v_max3_f32 v242, v242, v116, v117\n\t" \
  "v_mfma_f32_32x32x16_bf16 a[96:111], v[172:175], v[0:3], a[96:111]\n\t" \
  "v_exp_f32_e32 v56, v56\n\t" \
  "v_add_f32_e32 v233, v233, v47\n\t" \
  "v_cvt_pk_bf16_f32 v43, v46, v47\n\t" \
  "v_max3_f32 v241, v241, v102, v103\n\t" \
  "v_max3_f32 v242, v242, v118, v119\n\t" \
  "v_mfma_f32_32x32x16_bf16 a[112:127], v[172:175], v[16:19], a[112:127]\n\t" \
  "v_exp_f32_e32 v57, v57\n\t" \
  "v_add_f32_e32 v234, v234, v56\n\t" \
  "v_max3_f32 v241, v241, v104, v105\n\t" \
  "v_max3_f32 v242, v242, v120, v121\n\t" \
  "v_mfma_f32_32x32x16_bf16 a[0:15], v[176:179], v[8:11], a[0:15]\n\t" \
  "v_exp_f32_e32 v58, v58\n\t" \
  "v_add_f32_e32 v234, v234, v57\n\t" \
  "v_cvt_pk_bf16_f32 v56, v56, v57\n\t" \
  "v_max3_f32 v241, v241, v106, v107\n\t" \
  "v_max3_f32 v242, v242, v122, v123\n\t" \
  "v_mfma_f32_32x32x16_bf16 a[16:31], v[176:179], v[24:27], a[16:31]\n\t" \
  "v_exp_f32_e32 v59, v59\n\t" \
  "v_add_f32_e32 v234, v234, v58\n\t" \
  "v_max3_f32 v241, v241, v108, v109\n\t" \
  "v_max3_f32 v242, v242, v124, v125\n\t" \
  "v_mfma_f32_32x32x16_bf16 a[32:47], v[180:183], v[8:11], a[32:47]\n\t" \
  "v_exp_f32_e32 v60, v60\n\t" \
  "v_add_f32_e32 v234, v234, v59\n\t" \
  "v_cvt_pk_bf16_f32 v57, v58, v59\n\t" \
  "v_max3_f32 v241, v241, v110, v111\n\t" \
  "v_max3_f32 v242, v242, v126, v127\n\t" \
  "v_mfma_f32_32x32x16_bf16 a[48:63], v[180:183], v[24:27], a[48:63]\n\t" \
  "v_exp_f32_e32 v61, v61\n\t" \
  "v_add_f32_e32 v234, v234, v60\n\t" \
  "v_mov_b32_e32 v243, v241\n\t" \
  "v_mov_b32_e32 v244, v242\n\t" \
  "v_mfma_f32_32x32x16_bf16 a[64:79], v[184:187], v[8:11], a[64:79]\n\t" \
  "v_exp_f32_e32 v62, v62\n\t" \
  "v_add_f32_e32 v234, v234, v61\n\t" \
  "v_cvt_pk_bf16_f32 v58, v60, v61\n\t" \
  "v_permlane32_swap_b32_e32 v241, v243\n\t" \
  "v_permlane32_swap_b32_e32 v242, v244\n\t" \
  "v_mfma_f32_32x32x16_bf16 a[80:95], v[184:187], v[24:27], a[80:95]\n\t" \
  "v_exp_f32_e32 v63, v63\n\t" \
  "v_add_f32_e32 v234, v234, v62\n\t" \
  "v_max_f32_e32 v245, v241, v243\n\t" \
  "v_max_f32_e32 v246, v242, v244\n\t" \
  "v_max_f32_e32 v249, v245, v246\n\t" \
  "v_cmp_lt_f32_e32 vcc, 0x41000000, v249\n\t" \
  "s_cbranch_vccnz LTA1_%=\n\t" \
  "LBA1_%=:\n\t" \
  "v_mfma_f32_32x32x16_bf16 a[96:111], v[188:191], v[8:11], a[96:111]\n\t" \
  "ds_read_b64_tr_b16 v[160:161], v228 offset:16384\n\t" \
  "ds_read_b128 a[192:195], v224 offset:0\n\t" \
  "v_exp_f32_e32 v64, v64\n\t" \
  "v_add_f32_e32 v234, v234, v63\n\t" \
  "v_cvt_pk_bf16_f32 v59, v62, v63\n\t" \
  "v_mfma_f32_32x32x16_bf16 a[112:127], v[188:191], v[24:27], a[112:127]\n\t" \
  "ds_read_b64_tr_b16 v[162:163], v228 offset:18432\n\t" \
  "ds_read_b128 a[196:199], v225 offset:0\n\t" \
  "v_exp_f32_e32 v65, v65\n\t" \
  "v_mov_b32_e32 v235, v64\n\t" \
  "v_mfma_f32_32x32x16_bf16 a[0:15], v[192:195], v[32:35], a[0:15]\n\t" \
  "ds_read_b64_tr_b16 v[164:165], v228 offset:16896\n\t" \
  "ds_read_b128 a[200:203], v226 offset:0\n\t" \
  "v_exp_f32_e32 v66, v66\n\t" \
  "v_add_f32_e32 v235, v235, v65\n\t" \
  "v_cvt_pk_bf16_f32 v64, v64, v65\n\t" \
  "v_add_f32_e32 v239, v239, v233\n\t" \
  "v_mfma_f32_32x32x16_bf16 a[16:31], v[192:195], v[48:51], a[16:31]\n\t" \
  "ds_read_b64_tr_b16 v[166:167], v228 offset:18944\n\t" \
  "ds_read_b128 a[204:207], v227 offset:0\n\t" \
  "v_exp_f32_e32 v67, v67\n\t" \
  "v_add_f32_e32 v235, v235, v66\n\t" \
  "v_add_f32_e32 v240, v240, v234\n\t" \
  "v_mfma_f32_32x32x16_bf16 a[32:47], v[196:199], v[32:35], a[32:47]\n\t" \
  "ds_read_b64_tr_b16 v[168:169], v228 offset:17408\n\t" \
  "ds_read_b128 a[208:211], v224 offset:128\n\t" \
  "v_exp_f32_e32 v68, v68\n\t" \
  "v_add_f32_e32 v235, v235, v67\n\t" \
  "v_cvt_pk_bf16_f32 v65, v66, v67\n\t" \
  "v_mfma_f32_32x32x16_bf16 a[48:63], v[196:199], v[48:51], a[48:63]\n\t" \
  "ds_read_b64_tr_b16 v[170:171], v228 offset:19456\n\t" \
  "ds_read_b128 a[212:215], v225 offset:128\n\t" \
  "v_exp_f32_e32 v69, v69\n\t" \
  "v_add_f32_e32 v235, v235, v68\n\t" \
  "v_mfma_f32_32x32x16_bf16 a[64:79], v[200:203], v[32:35], a[64:79]\n\t" \
  "ds_read_b64_tr_b16 v[172:173], v228 offset:17920\n\t" \
  "ds_read_b128 a[216:219], v226 offset:128\n\t" \
  "v_exp_f32_e32 v70, v70\n\t" \
  "v_add_f32_e32 v235, v235, v69\n\t" \
  "v_cvt_pk_bf16_f32 v66, v68, v69\n\t" \
  "v_mfma_f32_32x32x16_bf16 a[80:95], v[200:203], v[48:51], a[80:95]\n\t" \
  "ds_read_b64_tr_b16 v[174:175], v228 offset:19968\n\t" \
  "ds_read_b128 a[220:223], v227 offset:128\n\t" \
  "v_exp_f32_e32 v71, v71\n\t" \
  "v_add_f32_e32 v235, v235, v70\n\t" \
  "v_mfma_f32_32x32x16_bf16 a[96:111], v[204:207], v[32:35], a[96:111]\n\t" \
  "ds_read_b64_tr_b16 v[176:177], v228 offset:20480\n\t" \
  "ds_read_b128 a[224:227], v224 offset:8192\n\t" \
  "v_exp_f32_e32 v80, v80\n\t" \
  "v_add_f32_e32 v235, v235, v71\n\t" \
  "v_cvt_pk_bf16_f32 v67, v70, v71\n\t" \
  "v_mfma_f32_32x32x16_bf16 a[112:127], v[204:207], v[48:51], a[112:127]\n\t" \
  "ds_read_b64_tr_b16 v[178:179], v228 offset:22528\n\t" \
  "ds_read_b128 a[228:231], v225 offset:8192\n\t" \
  "v_exp_f32_e32 v81, v81\n\t" \
  "v_mov_b32_e32 v236, v80\n\t" \
  "v_mfma_f32_32x32x16_bf16 a[0:15], v[208:211], v[40:43], a[0:15]\n\t" \
  "ds_read_b64_tr_b16 v[180:181], v228 offset:20992\n\t" \
  "ds_read_b128 a[232:235], v226 offset:8192\n\t" \
  "v_exp_f32_e32 v82, v82\n\t" \
  "v_add_f32_e32 v236, v236, v81\n\t" \
  "v_cvt_pk_bf16_f32 v80, v80, v81\n\t" \
  "v_mfma_f32_32x32x16_bf16 a[16:31], v[208:211], v[56:59], a[16:31]\n\t" \
  "ds_read_b64_tr_b16 v[182:183], v228 offset:23040\n\t" \
  "ds_read_b128 a[236:239], v227 offset:8192\n\t" \
  "v_exp_f32_e32 v83, v83\n\t" \
  "v_add_f32_e32 v236, v236, v82\n\t" \
  "v_mfma_f32_32x32x16_bf16 a[32:47], v[212:215], v[40:43], a[32:47]\n\t" \
  "ds_read_b64_tr_b16 v[184:185], v228 offset:21504\n\t" \
  "ds_read_b128 a[240:243], v224 offset:8320\n\t" \
  "v_exp_f32_e32 v84, v84\n\t" \
  "v_add_f32_e32 v236, v236, v83\n\t" \
  "v_cvt_pk_bf16_f32 v81, v82, v83\n\t" \
  "v_mfma_f32_32x32x16_bf16 a[48:63], v[212:215], v[56:59], a[48:63]\n\t" \
  "ds_read_b64_tr_b16 v[186:187], v228 offset:23552\n\t" \
  "ds_read_b128 a[244:247], v225 offset:8320\n\t" \
  "v_exp_f32_e32 v85, v85\n\t" \
  "v_add_f32_e32 v236, v236, v84\n\t" \
  "v_mfma_f32_32x32x16_bf16 a[64:79], v[216:219], v[40:43], a[64:79]\n\t" \
  "ds_read_b64_tr_b16 v[188:189], v228 offset:22016\n\t" \
  "ds_read_b128 a[248:251], v226 offset:8320\n\t" \
  "v_exp_f32_e32 v86, v86\n\t" \
  "v_add_f32_e32 v236, v236, v85\n\t" \
  "v_cvt_pk_bf16_f32 v82, v84, v85\n\t" \
  "v_mfma_f32_32x32x16_bf16 a[80:95], v[216:219], v[56:59], a[80:95]\n\t" \
  "ds_read_b64_tr_b16 v[190:191], v228 offset:24064\n\t" \
  "ds_read_b128 a[252:255], v227 offset:8320\n\t" \
  "v_exp_f32_e32 v87, v87\n\t" \
  "v_add_f32_e32 v236, v236, v86\n\t" \
  "v_mfma_f32_32x32x16_bf16 a[96:111], v[220:223], v[40:43], a[96:111]\n\t" \
  "ds_read_b64_tr_b16 v[192:193], v228 offset:24576\n\t" \
  "v_exp_f32_e32 v72, v72\n\t" \
  "v_add_f32_e32 v236, v236, v87\n\t" \
  "v_cvt_pk_bf16_f32 v83, v86, v87\n\t" \
  "v_mfma_f32_32x32x16_bf16 a[112:127], v[220:223], v[56:59], a[112:127]\n\t" \
  "ds_read_b64_tr_b16 v[194:195], v228 offset:26624\n\t" \
  "v_exp_f32_e32 v73, v73\n\t" \
  "v_add_f32_e32 v235, v235, v72\n\t" \
  "s_cmp_lg_u32 s40, 0\n\t" \
  "s_cbranch_scc1 LTB1_%=\n\t" \
  "LBB1_%=:\n\t" \
  "s_sub_u32 s41, s41, 1\n\t" \
  "s_cmp_eq_u32 s41, 0\n\t" \
  "s_cbranch_scc1 LDRAIN_%=\n\t" \
  "s_waitcnt lgkmcnt(10)\n\t" \
  "v_mfma_f32_32x32x16_bf16 v[0:15], a[192:195], a[128:131], v[128:143]\n\t" \
  "ds_read_b64_tr_b16 v[196:197], v228 offset:25088\n\t" \
  "v_exp_f32_e32 v74, v74\n\t" \
  "v_add_f32_e32 v235, v235, v73\n\t" \
  "v_cvt_pk_bf16_f32 v72, v72, v73\n\t" \
  "v_mfma_f32_32x32x16_bf16 v[16:31], a[192:195], a[160:163], v[144:159]\n\t" \
  "ds_read_b64_tr_b16 v[198:199], v228 offset:27136\n\t" \
  "v_exp_f32_e32 v75, v75\n\t" \
  "v_add_f32_e32 v235, v235, v74\n\t" \
  "v_mfma_f32_32x32x16_bf16 v[0:15], a[196:199], a[132:135], v[0:15]\n\t" \
  "ds_read_b64_tr_b16 v[200:201], v228 offset:25600\n\t" \
  "v_exp_f32_e32 v76, v76\n\t" \
  "v_add_f32_e32 v235, v235, v75\n\t" \
  "v_cvt_pk_bf16_f32 v73, v74, v75\n\t" \
  "v_mfma_f32_32x32x16_bf16 v[16:31], a[196:199], a[164:167], v[16:31]\n\t" \
  "ds_read_b64_tr_b16 v[202:203], v228 offset:27648\n\t" \
  "v_exp_f32_e32 v77, v77\n\t" \
  "v_add_f32_e32 v235, v235, v76\n\t" \
  "v_mfma_f32_32x32x16_bf16 v[0:15], a[200:203], a[136:139], v[0:15]\n\t" \
  "ds_read_b64_tr_b16 v[204:205], v228 offset:26112\n\t" \
  "v_exp_f32_e32 v78, v78\n\t" \
  "v_add_f32_e32 v235, v235, v77\n\t" \
  "v_cvt_pk_bf16_f32 v74, v76, v77\n\t" \
  "v_mfma_f32_32x32x16_bf16 v[16:31], a[200:203], a[168:171], v[16:31]\n\t" \
  "ds_read_b64_tr_b16 v[206:207], v228 offset:28160\n\t" \
  "v_exp_f32_e32 v79, v79\n\t" \
  "v_add_f32_e32 v235, v235, v78\n\t" \
  "v_mfma_f32_32x32x16_bf16 v[0:15], a[204:207], a[140:143], v[0:15]\n\t" \
  "ds_read_b64_tr_b16 v[208:209], v228 offset:28672\n\t" \
  "v_exp_f32_e32 v88, v88\n\t" \
  "v_add_f32_e32 v235, v235, v79\n\t" \
  "v_cvt_pk_bf16_f32 v75, v78, v79\n\t" \
  "v_mfma_f32_32x32x16_bf16 v[16:31], a[204:207], a[172:175], v[16:31]\n\t" \
  "ds_read_b64_tr_b16 v[210:211], v228 offset:30720\n\t" \
  "v_exp_f32_e32 v89, v89\n\t" \
  "v_add_f32_e32 v236, v236, v88\n\t" \
  "v_mfma_f32_32x32x16_bf16 v[0:15], a[208:211], a[144:147], v[0:15]\n\t" \
  "ds_read_b64_tr_b16 v[212:213], v228 offset:29184\n\t" \
  "v_exp_f32_e32 v90, v90\n\t" \
  "v_add_f32_e32 v236, v236, v89\n\t" \
  "v_cvt_pk_bf16_f32 v88, v88, v89\n\t" \
  "v_mfma_f32_32x32x16_bf16 v[16:31], a[208:211], a[176:179], v[16:31]\n\t" \
  "ds_read_b64_tr_b16 v[214:215], v228 offset:31232\n\t" \
  "v_exp_f32_e32 v91, v91\n\t" \
  "v_add_f32_e32 v236, v236, v90\n\t" \
  "v_mfma_f32_32x32x16_bf16 v[0:15], a[212:215], a[148:151], v[0:15]\n\t" \
  "ds_read_b64_tr_b16 v[216:217], v228 offset:29696\n\t" \
  "v_exp_f32_e32 v92, v92\n\t" \
  "v_add_f32_e32 v236, v236, v91\n\t" \
  "v_cvt_pk_bf16_f32 v89, v90, v91\n\t" \
  "v_mfma_f32_32x32x16_bf16 v[16:31], a[212:215], a[180:183], v[16:31]\n\t" \
  "ds_read_b64_tr_b16 v[218:219], v228 offset:31744\n\t" \
  "v_exp_f32_e32 v93, v93\n\t" \
  "v_add_f32_e32 v236, v236, v92\n\t" \
  "v_mfma_f32_32x32x16_bf16 v[0:15], a[216:219], a[152:155], v[0:15]\n\t" \
  "ds_read_b64_tr_b16 v[220:221], v228 offset:30208\n\t" \
  "v_exp_f32_e32 v94, v94\n\t" \
  "v_add_f32_e32 v236, v236, v93\n\t" \
  "v_cvt_pk_bf16_f32 v90, v92, v93\n\t" \
  "v_mfma_f32_32x32x16_bf16 v[16:31], a[216:219], a[184:187], v[16:31]\n\t" \
  "ds_read_b64_tr_b16 v[222:223], v228 offset:32256\n\t" \
  "v_exp_f32_e32 v95, v95\n\t" \
  "v_add_f32_e32 v236, v236, v94\n\t" \
  "v_mfma_f32_32x32x16_bf16 v[0:15], a[220:223], a[156:159], v[0:15]\n\t" \
  "v_exp_f32_e32 v96, v96\n\t" \
  "v_add_f32_e32 v236, v236, v95\n\t" \
  "v_cvt_pk_bf16_f32 v91, v94, v95\n\t" \
  "s_xor_b32 %[m0k], %[m0k], 0x8000\n\t" \
  "v_mfma_f32_32x32x16_bf16 v[16:31], a[220:223], a[188:191], v[16:31]\n\t" \
  "v_exp_f32_e32 v97, v97\n\t" \
  "v_add_f32_e32 v235, v235, v96\n\t" \
  "s_add_u32 m0, %[m0k], 0\n\t" \
  "s_waitcnt vmcnt(16) lgkmcnt(0)\n\t" \
  "s_barrier\n\t" \
  "v_mfma_f32_32x32x16_bf16 v[32:47], a[224:227], a[128:131], v[128:143]\n\t" \
  "buffer_load_dwordx4 v229, %[srdk], %[koff] offen nt lds\n\t" \
  "v_exp_f32_e32 v98, v98\n\t" \
  "v_add_f32_e32 v235, v235, v97\n\t" \
  "v_cvt_pk_bf16_f32 v96, v96, v97\n\t" \
  "v_mfma_f32_32x32x16_bf16 v[48:63], a[224:227], a[160:163], v[144:159]\n\t" \
  "v_exp_f32_e32 v99, v99\n\t" \
  "v_add_f32_e32 v235, v235, v98\n\t" \
  "v_mfma_f32_32x32x16_bf16 v[32:47], a[228:231], a[132:135], v[32:47]\n\t" \
  "buffer_load_dwordx4 v230, %[srdk], %[koff] offen offset:1024 nt lds\n\t" \
  "v_exp_f32_e32 v100, v100\n\t" \
  "v_add_f32_e32 v235, v235, v99\n\t" \
  "v_cvt_pk_bf16_f32 v97, v98, v99\n\t" \
  "v_mfma_f32_32x32x16_bf16 v[48:63], a[228:231], a[164:167], v[48:63]\n\t" \
  "v_exp_f32_e32 v101, v101\n\t" \
  "v_add_f32_e32 v235, v235, v100\n\t" \
  "v_max_f32_e32 v241, v0, v1\n\t" \
  "v_max_f32_e32 v242, v16, v17\n\t" \
  "v_mfma_f32_32x32x16_bf16 v[32:47], a[232:235], a[136:139], v[32:47]\n\t" \
  "buffer_load_dwordx4 v229, %[srdk], %[koff] offen offset:2048 nt lds\n\t" \
  "v_exp_f32_e32 v102, v102\n\t" \
  "v_add_f32_e32 v235, v235, v101\n\t" \
  "v_cvt_pk_bf16_f32 v98, v100, v101\n\t" \
  "v_mfma_f32_32x32x16_bf16 v[48:63], a[232:235], a[168:171], v[48:63]\n\t" \
  "v_exp_f32_e32 v103, v103\n\t" \
  "v_add_f32_e32 v235, v235, v102\n\t" \
  "v_max3_f32 v241, v241, v2, v3\n\t" \
  "v_max3_f32 v242, v242, v18, v19\n\t" \
  "v_mfma_f32_32x32x16_bf16 v[32:47], a[236:239], a[140:143], v[32:47]\n\t" \
  "buffer_load_dwordx4 v230, %[srdk], %[koff] offen offset:3072 nt lds\n\t" \
  "v_exp_f32_e32 v112, v112\n\t" \
  "v_add_f32_e32 v235, v235, v103\n\t" \
  "v_cvt_pk_bf16_f32 v99, v102, v103\n\t" \
  "v_mfma_f32_32x32x16_bf16 v[48:63], a[236:239], a[172:175], v[48:63]\n\t" \
  "v_exp_f32_e32 v113, v113\n\t" \
  "v_add_f32_e32 v236, v236, v112\n\t" \
  "v_max3_f32 v241, v241, v4, v5\n\t" \
  "v_max3_f32 v242, v242, v20, v21\n\t" \
  "s_add_u32 m0, %[m0v], 16384\n\t" \
  "v_mfma_f32_32x32x16_bf16 v[32:47], a[240:243], a[144:147], v[32:47]\n\t" \
  "buffer_load_dwordx4 v231, %[srdv], %[voff] offen nt lds\n\t" \
  "v_exp_f32_e32 v114, v114\n\t" \
  "v_add_f32_e32 v236, v236, v113\n\t" \
  "v_cvt_pk_bf16_f32 v112, v112, v113\n\t" \
  "v_mfma_f32_32x32x16_bf16 v[48:63], a[240:243], a[176:179], v[48:63]\n\t" \
  "v_exp_f32_e32 v115, v115\n\t" \
  "v_add_f32_e32 v236, v236, v114\n\t" \
  "v_max3_f32 v241, v241, v6, v7\n\t" \
  "v_max3_f32 v242, v242, v22, v23\n\t" \
  "v_mfma_f32_32x32x16_bf16 v[32:47], a[244:247], a[148:151], v[32:47]\n\t" \
  "buffer_load_dwordx4 v231, %[srdv], %[voff] offen offset:2048 nt lds\n\t" \
  "v_exp_f32_e32 v116, v116\n\t" \
  "v_add_f32_e32 v236, v236, v115\n\t" \
  "v_cvt_pk_bf16_f32 v113, v114, v115\n\t" \
  "v_mfma_f32_32x32x16_bf16 v[48:63], a[244:247], a[180:183], v[48:63]\n\t" \
  "v_exp_f32_e32 v117, v117\n\t" \
  "v_add_f32_e32 v236, v236, v116\n\t" \
  "v_max3_f32 v241, v241, v8, v9\n\t" \
  "v_max3_f32 v242, v242, v24, v25\n\t" \
  "s_add_u32 m0, %[m0v], 20480\n\t" \
  "v_mfma_f32_32x32x16_bf16 v[32:47], a[248:251], a[152:155], v[32:47]\n\t" \
  "buffer_load_dwordx4 v232, %[srdv], %[voff] offen nt lds\n\t" \
  "v_exp_f32_e32 v118, v118\n\t" \
  "v_add_f32_e32 v236, v236, v117\n\t" \
  "v_cvt_pk_bf16_f32 v114, v116, v117\n\t" \
  "v_mfma_f32_32x32x16_bf16 v[48:63], a[248:251], a[184:187], v[48:63]\n\t" \
  "v_exp_f32_e32 v119, v119\n\t" \
  "v_add_f32_e32 v236, v236, v118\n\t" \
  "v_max3_f32 v241, v241, v10, v11\n\t" \
  "v_max3_f32 v242, v242, v26, v27\n\t" \
  "v_mfma_f32_32x32x16_bf16 v[32:47], a[252:255], a[156:159], v[32:47]\n\t" \
  "buffer_load_dwordx4 v232, %[srdv], %[voff] offen offset:2048 nt lds\n\t" \
  "v_exp_f32_e32 v104, v104\n\t" \
  "v_add_f32_e32 v236, v236, v119\n\t" \
  "v_cvt_pk_bf16_f32 v115, v118, v119\n\t" \
  "v_mfma_f32_32x32x16_bf16 v[48:63], a[252:255], a[188:191], v[48:63]\n\t" \
  "v_exp_f32_e32 v105, v105\n\t" \
  "v_add_f32_e32 v235, v235, v104\n\t" \
  "v_max3_f32 v241, v241, v12, v13\n\t" \
  "v_max3_f32 v242, v242, v28, v29\n\t" \
  "v_mfma_f32_32x32x16_bf16 a[0:15], v[160:163], v[64:67], a[0:15]\n\t" \
  "v_exp_f32_e32 v106, v106\n\t" \
  "v_add_f32_e32 v235, v235, v105\n\t" \
  "v_cvt_pk_bf16_f32 v104, v104, v105\n\t" \
  "v_max3_f32 v241, v241, v14, v15\n\t" \
  "s_add_u32 %[koff], %[koff], 16384\n\t" \
  "v_mfma_f32_32x32x16_bf16 a[16:31], v[160:163], v[80:83], a[16:31]\n\t" \
  "v_exp_f32_e32 v107, v107\n\t" \
  "v_add_f32_e32 v235, v235, v106\n\t" \
  "v_max3_f32 v242, v242, v30, v31\n\t" \
  "s_add_u32 %[voff], %[voff], 16384\n\t" \
  "v_mfma_f32_32x32x16_bf16 a[32:47], v[164:167], v[64:67], a[32:47]\n\t" \
  "v_exp_f32_e32 v108, v108\n\t" \
  "v_add_f32_e32 v235, v235, v107\n\t" \
  "v_cvt_pk_bf16_f32 v105, v106, v107\n\t" \
  "v_xor_b32_e32 v228, 0x8000, v228\n\t" \
  "s_xor_b32 %[m0v], %[m0v], 0x8000\n\t" \
  "v_mfma_f32_32x32x16_bf16 a[48:63], v[164:167], v[80:83], a[48:63]\n\t" \
  "v_exp_f32_e32 v109, v109\n\t" \
  "v_add_f32_e32 v235, v235, v108\n\t" \
  "v_max3_f32 v241, v241, v32, v33\n\t" \
  "v_max3_f32 v242, v242, v48, v49\n\t" \
  "v_mfma_f32_32x32x16_bf16 a[64:79], v[168:171], v[64:67], a[64:79]\n\t" \
  "v_exp_f32_e32 v110, v110\n\t" \
  "v_add_f32_e32 v235, v235, v109\n\t" \
  "v_cvt_pk_bf16_f32 v106, v108, v109\n\t" \
  "v_max3_f32 v241, v241, v34, v35\n\t" \
  "v_max3_f32 v242, v242, v50, v51\n\t" \
  "v_mfma_f32_32x32x16_bf16 a[80:95], v[168:171], v[80:83], a[80:95]\n\t" \
  "v_exp_f32_e32 v111, v111\n\t" \
  "v_add_f32_e32 v235, v235, v110\n\t" \
  "v_max3_f32 v241, v241, v36, v37\n\t" \
  "v_max3_f32 v242, v242, v52, v53\n\t" \
  "v_mfma_f32_32x32x16_bf16 a[96:111], v[172:175], v[64:67], a[96:111]\n\t" \
  "v_exp_f32_e32 v120, v120\n\t" \
  "v_add_f32_e32 v235, v235, v111\n\t" \
  "v_cvt_pk_bf16_f32 v107, v110, v111\n\t" \
  "v_max3_f32 v241, v241, v38, v39\n\t" \
  "v_max3_f32 v242, v242, v54, v55\n\t" \
  "v_mfma_f32_32x32x16_bf16 a[112:127], v[172:175], v[80:83], a[112:127]\n\t" \
  "v_exp_f32_e32 v121, v121\n\t" \
  "v_add_f32_e32 v236, v236, v120\n\t" \
  "v_max3_f32 v241, v241, v40, v41\n\t" \
  "v_max3_f32 v242, v242, v56, v57\n\t" \
  "v_mfma_f32_32x32x16_bf16 a[0:15], v[176:179], v[72:75], a[0:15]\n\t" \
  "v_exp_f32_e32 v122, v122\n\t" \
  "v_add_f32_e32 v236, v236, v121\n\t" \
  "v_cvt_pk_bf16_f32 v120, v120, v121\n\t" \
  "v_max3_f32 v241, v241, v42, v43\n\t" \
  "v_max3_f32 v242, v242, v58, v59\n\t" \
  "v_mfma_f32_32x32x16_bf16 a[16:31], v[176:179], v[88:91], a[16:31]\n\t" \
  "v_exp_f32_e32 v123, v123\n\t" \
  "v_add_f32_e32 v236, v236, v122\n\t" \
  "v_max3_f32 v241, v241, v44, v45\n\t" \
  "v_max3_f32 v242, v242, v60, v61\n\t" \
  "v_mfma_f32_32x32x16_bf16 a[32:47], v[180:183], v[72:75], a[32:47]\n\t" \
  "v_exp_f32_e32 v124, v124\n\t" \
  "v_add_f32_e32 v236, v236, v123\n\t" \
  "v_cvt_pk_bf16_f32 v121, v122, v123\n\t" \
  "v_max3_f32 v241, v241, v46, v47\n\t" \
  "v_max3_f32 v242, v242, v62, v63\n\t" \
  "v_mfma_f32_32x32x16_bf16 a[48:63], v[180:183], v[88:91], a[48:63]\n\t" \
  "v_exp_f32_e32 v125, v125\n\t" \
  "v_add_f32_e32 v236, v236, v124\n\t" \
  "v_mov_b32_e32 v243, v241\n\t" \
  "v_mov_b32_e32 v244, v242\n\t" \
  "v_mfma_f32_32x32x16_bf16 a[64:79], v[184:187], v[72:75], a[64:79]\n\t" \
  "v_exp_f32_e32 v126, v126\n\t" \
  "v_add_f32_e32 v236, v236, v125\n\t" \
  "v_cvt_pk_bf16_f32 v122, v124, v125\n\t" \
  "v_permlane32_swap_b32_e32 v241, v243\n\t" \
  "v_permlane32_swap_b32_e32 v242, v244\n\t" \
  "v_mfma_f32_32x32x16_bf16 a[80:95], v[184:187], v[88:91], a[80:95]\n\t" \
  "v_exp_f32_e32 v127, v127\n\t" \
  "v_add_f32_e32 v236, v236, v126\n\t" \
  "v_max_f32_e32 v245, v241, v243\n\t" \
  "v_max_f32_e32 v246, v242, v244\n\t" \
  "v_max_f32_e32 v249, v245, v246\n\t" \
  "v_cmp_lt_f32_e32 vcc, 0x41000000, v249\n\t" \
  "s_cbranch_vccnz LTA2_%=\n\t" \
  "LBA2_%=:\n\t" \
  "v_mfma_f32_32x32x16_bf16 a[96:111], v[188:191], v[72:75], a[96:111]\n\t" \
  "ds_read_b64_tr_b16 v[160:161], v228 offset:0\n\t" \
  "ds_read_b128 a[192:195], v224 offset:16384\n\t" \
  "v_exp_f32_e32 v0, v0\n\t" \
  "v_add_f32_e32 v236, v236, v127\n\t" \
  "v_cvt_pk_bf16_f32 v123, v126, v127\n\t" \
  "v_mfma_f32_32x32x16_bf16 a[112:127], v[188:191], v[88:91], a[112:127]\n\t" \
  "ds_read_b64_tr_b16 v[162:163], v228 offset:2048\n\t" \
  "ds_read_b128 a[196:199], v225 offset:16384\n\t" \
  "v_exp_f32_e32 v1, v1\n\t" \
  "v_mov_b32_e32 v233, v0\n\t" \
  "v_mfma_f32_32x32x16_bf16 a[0:15], v[192:195], v[96:99], a[0:15]\n\t" \
  "ds_read_b64_tr_b16 v[164:165], v228 offset:512\n\t" \
  "ds_read_b128 a[200:203], v226 offset:16384\n\t" \
  "v_exp_f32_e32 v2, v2\n\t" \
  "v_add_f32_e32 v233, v233, v1\n\t" \
  "v_cvt_pk_bf16_f32 v0, v0, v1\n\t" \
  "v_add_f32_e32 v239, v239, v235\n\t" \
  "v_mfma_f32_32x32x16_bf16 a[16:31], v[192:195], v[112:115], a[16:31]\n\t" \
  "ds_read_b64_tr_b16 v[166:167], v228 offset:2560\n\t" \
  "ds_read_b128 a[204:207], v227 offset:16384\n\t" \
  "v_exp_f32_e32 v3, v3\n\t" \
  "v_add_f32_e32 v233, v233, v2\n\t" \
  "v_add_f32_e32 v240, v240, v236\n\t" \
  "v_mfma_f32_32x32x16_bf16 a[32:47], v[196:199], v[96:99], a[32:47]\n\t" \
  "ds_read_b64_tr_b16 v[168:169], v228 offset:1024\n\t" \
  "ds_read_b128 a[208:211], v224 offset:16512\n\t" \
  "v_exp_f32_e32 v4, v4\n\t" \
  "v_add_f32_e32 v233, v233, v3\n\t" \
  "v_cvt_pk_bf16_f32 v1, v2, v3\n\t" \
  "v_mfma_f32_32x32x16_bf16 a[48:63], v[196:199], v[112:115], a[48:63]\n\t" \
  "ds_read_b64_tr_b16 v[170:171], v228 offset:3072\n\t" \
  "ds_read_b128 a[212:215], v225 offset:16512\n\t" \
  "v_exp_f32_e32 v5, v5\n\t" \
  "v_add_f32_e32 v233, v233, v4\n\t" \
  "v_mfma_f32_32x32x16_bf16 a[64:79], v[200:203], v[96:99], a[64:79]\n\t" \
  "ds_read_b64_tr_b16 v[172:173], v228 offset:1536\n\t" \
  "ds_read_b128 a[216:219], v226 offset:16512\n\t" \
  "v_exp_f32_e32 v6, v6\n\t" \
  "v_add_f32_e32 v233, v233, v5\n\t" \
  "v_cvt_pk_bf16_f32 v2, v4, v5\n\t" \
  "v_mfma_f32_32x32x16_bf16 a[80:95], v[200:203], v[112:115], a[80:95]\n\t" \
  "ds_read_b64_tr_b16 v[174:175], v228 offset:3584\n\t" \
  "ds_read_b128 a[220:223], v227 offset:16512\n\t" \
  "v_exp_f32_e32 v7, v7\n\t" \
  "v_add_f32_e32 v233, v233, v6\n\t" \
  "v_mfma_f32_32x32x16_bf16 a[96:111], v[204:207], v[96:99], a[96:111]\n\t" \
  "ds_read_b64_tr_b16 v[176:177], v228 offset:4096\n\t" \
  "ds_read_b128 a[224:227], v224 offset:24576\n\t" \
  "v_exp_f32_e32 v16, v16\n\t" \
  "v_add_f32_e32 v233, v233, v7\n\t" \
  "v_cvt_pk_bf16_f32 v3, v6, v7\n\t" \
  "v_mfma_f32_32x32x16_bf16 a[112:127], v[204:207], v[112:115], a[112:127]\n\t" \
  "ds_read_b64_tr_b16 v[178:179], v228 offset:6144\n\t" \
  "ds_read_b128 a[228:231], v225 offset:24576\n\t" \
  "v_exp_f32_e32 v17, v17\n\t" \
  "v_mov_b32_e32 v234, v16\n\t" \
  "v_mfma_f32_32x32x16_bf16 a[0:15], v[208:211], v[104:107], a[0:15]\n\t" \
  "ds_read_b64_tr_b16 v[180:181], v228 offset:4608\n\t" \
  "ds_read_b128 a[232:235], v226 offset:24576\n\t" \
  "v_exp_f32_e32 v18, v18\n\t" \
  "v_add_f32_e32 v234, v234, v17\n\t" \
  "v_cvt_pk_bf16_f32 v16, v16, v17\n\t" \
  "v_mfma_f32_32x32x16_bf16 a[16:31], v[208:211], v[120:123], a[16:31]\n\t" \
  "ds_read_b64_tr_b16 v[182:183], v228 offset:6656\n\t" \
  "ds_read_b128 a[236:239], v227 offset:24576\n\t" \
  "v_exp_f32_e32 v19, v19\n\t" \
  "v_add_f32_e32 v234, v234, v18\n\t" \
  "v_mfma_f32_32x32x16_bf16 a[32:47], v[212:215], v[104:107], a[32:47]\n\t" \
  "ds_read_b64_tr_b16 v[184:185], v228 offset:5120\n\t" \
  "ds_read_b128 a[240:243], v224 offset:24704\n\t" \
  "v_exp_f32_e32 v20, v20\n\t" \
  "v_add_f32_e32 v234, v234, v19\n\t" \
  "v_cvt_pk_bf16_f32 v17, v18, v19\n\t" \
  "v_mfma_f32_32x32x16_bf16 a[48:63], v[212:215], v[120:123], a[48:63]\n\t" \
  "ds_read_b64_tr_b16 v[186:187], v228 offset:7168\n\t" \
  "ds_read_b128 a[244:247], v225 offset:24704\n\t" \
  "v_exp_f32_e32 v21, v21\n\t" \
  "v_add_f32_e32 v234, v234, v20\n\t" \
  "v_mfma_f32_32x32x16_bf16 a[64:79], v[216:219], v[104:107], a[64:79]\n\t" \
  "ds_read_b64_tr_b16 v[188:189], v228 offset:5632\n\t" \
  "ds_read_b128 a[248:251], v226 offset:24704\n\t" \
  "v_exp_f32_e32 v22, v22\n\t" \
  "v_add_f32_e32 v234, v234, v21\n\t" \
  "v_cvt_pk_bf16_f32 v18, v20, v21\n\t" \
  "v_mfma_f32_32x32x16_bf16 a[80:95], v[216:219], v[120:123], a[80:95]\n\t" \
  "ds_read_b64_tr_b16 v[190:191], v228 offset:7680\n\t" \
  "ds_read_b128 a[252:255], v227 offset:24704\n\t" \
  "v_exp_f32_e32 v23, v23\n\t" \
  "v_add_f32_e32 v234, v234, v22\n\t" \
  "v_mfma_f32_32x32x16_bf16 a[96:111], v[220:223], v[104:107], a[96:111]\n\t" \
  "ds_read_b64_tr_b16 v[192:193], v228 offset:8192\n\t" \
  "v_exp_f32_e32 v8, v8\n\t" \
  "v_add_f32_e32 v234, v234, v23\n\t" \
  "v_cvt_pk_bf16_f32 v19, v22, v23\n\t" \
  "v_mfma_f32_32x32x16_bf16 a[112:127], v[220:223], v[120:123], a[112:127]\n\t" \
  "ds_read_b64_tr_b16 v[194:195], v228 offset:10240\n\t" \
  "v_exp_f32_e32 v9, v9\n\t" \
  "v_add_f32_e32 v233, v233, v8\n\t" \
  "s_cmp_lg_u32 s40, 0\n\t" \
  "s_cbranch_scc1 LTB2_%=\n\t" \
  "LBB2_%=:\n\t" \
  "s_branch LLOOP_%=\n\t" \
  "LDRAIN_%=:\n\t" \
  "ds_read_b64_tr_b16 v[196:197], v228 offset:25088\n\t" \
  "ds_read_b64_tr_b16 v[198:199], v228 offset:27136\n\t" \
  "ds_read_b64_tr_b16 v[200:201], v228 offset:25600\n\t" \
  "ds_read_b64_tr_b16 v[202:203], v228 offset:27648\n\t" \
  "ds_read_b64_tr_b16 v[204:205], v228 offset:26112\n\t" \
  "ds_read_b64_tr_b16 v[206:207], v228 offset:28160\n\t" \
  "ds_read_b64_tr_b16 v[208:209], v228 offset:28672\n\t" \
  "ds_read_b64_tr_b16 v[210:211], v228 offset:30720\n\t" \
  "ds_read_b64_tr_b16 v[212:213], v228 offset:29184\n\t" \
  "ds_read_b64_tr_b16 v[214:215], v228 offset:31232\n\t" \
  "ds_read_b64_tr_b16 v[216:217], v228 offset:29696\n\t" \
  "ds_read_b64_tr_b16 v[218:219], v228 offset:31744\n\t" \
  "ds_read_b64_tr_b16 v[220:221], v228 offset:30208\n\t" \
  "ds_read_b64_tr_b16 v[222:223], v228 offset:32256\n\t" \
  "s_waitcnt lgkmcnt(14)\n\t" \
  "v_mfma_f32_32x32x16_bf16 a[0:15], v[160:163], v[64:67], a[0:15]\n\t" \
  "v_exp_f32_e32 v74, v74\n\t" \
  "v_add_f32_e32 v235, v235, v73\n\t" \
  "v_cvt_pk_bf16_f32 v72, v72, v73\n\t" \
  "v_exp_f32_e32 v75, v75\n\t" \
  "v_mfma_f32_32x32x16_bf16 a[16:31], v[160:163], v[80:83], a[16:31]\n\t" \
  "v_add_f32_e32 v235, v235, v74\n\t" \
  "v_exp_f32_e32 v76, v76\n\t" \
  "v_add_f32_e32 v235, v235, v75\n\t" \
  "v_cvt_pk_bf16_f32 v73, v74, v75\n\t" \
  "v_exp_f32_e32 v77, v77\n\t" \
  "v_mfma_f32_32x32x16_bf16 a[32:47], v[164:167], v[64:67], a[32:47]\n\t" \
  "v_add_f32_e32 v235, v235, v76\n\t" \
  "v_exp_f32_e32 v78, v78\n\t" \
  "v_add_f32_e32 v235, v235, v77\n\t" \
  "v_cvt_pk_bf16_f32 v74, v76, v77\n\t" \
  "v_exp_f32_e32 v79, v79\n\t" \
  "v_mfma_f32_32x32x16_bf16 a[48:63], v[164:167], v[80:83], a[48:63]\n\t" \
  "v_add_f32_e32 v235, v235, v78\n\t" \
  "v_exp_f32_e32 v88, v88\n\t" \
  "v_add_f32_e32 v235, v235, v79\n\t" \
  "v_cvt_pk_bf16_f32 v75, v78, v79\n\t" \
  "v_exp_f32_e32 v89, v89\n\t" \
  "v_mfma_f32_32x32x16_bf16 a[64:79], v[168:171], v[64:67], a[64:79]\n\t" \
  "v_add_f32_e32 v236, v236, v88\n\t" \
  "v_exp_f32_e32 v90, v90\n\t" \
  "v_add_f32_e32 v236, v236, v89\n\t" \
  "v_cvt_pk_bf16_f32 v88, v88, v89\n\t" \
  "v_exp_f32_e32 v91, v91\n\t" \
  "v_mfma_f32_32x32x16_bf16 a[80:95], v[168:171], v[80:83], a[80:95]\n\t" \
  "v_add_f32_e32 v236, v236, v90\n\t" \
  "v_exp_f32_e32 v92, v92\n\t" \
  "v_add_f32_e32 v236, v236, v91\n\t" \
  "v_cvt_pk_bf16_f32 v89, v90, v91\n\t" \
  "v_exp_f32_e32 v93, v93\n\t" \
  "v_mfma_f32_32x32x16_bf16 a[96:111], v[172:175], v[64:67], a[96:111]\n\t" \
  "v_add_f32_e32 v236, v236, v92\n\t" \
  "v_exp_f32_e32 v94, v94\n\t" \
  "v_add_f32_e32 v236, v236, v93\n\t" \
  "v_cvt_pk_bf16_f32 v90, v92, v93\n\t" \
  "v_exp_f32_e32 v95, v95\n\t" \
  "v_mfma_f32_32x32x16_bf16 a[112:127], v[172:175], v[80:83], a[112:127]\n\t" \
  "v_add_f32_e32 v236, v236, v94\n\t" \
  "v_exp_f32_e32 v96, v96\n\t" \
  "v_add_f32_e32 v236, v236, v95\n\t" \
  "v_cvt_pk_bf16_f32 v91, v94, v95\n\t" \
  "v_exp_f32_e32 v97, v97\n\t" \
  "s_nop 1\n\t" \
  "s_waitcnt lgkmcnt(0)\n\t" \
  "v_mfma_f32_32x32x16_bf16 a[0:15], v[176:179], v[72:75], a[0:15]\n\t" \
  "v_add_f32_e32 v235, v235, v96\n\t" \
  "v_exp_f32_e32 v98, v98\n\t" \
  "v_add_f32_e32 v235, v235, v97\n\t" \
  "v_cvt_pk_bf16_f32 v96, v96, v97\n\t" \
  "v_exp_f32_e32 v99, v99\n\t" \
  "v_mfma_f32_32x32x16_bf16 a[16:31], v[176:179], v[88:91], a[16:31]\n\t" \
  "v_add_f32_e32 v235, v235, v98\n\t" \
  "v_exp_f32_e32 v100, v100\n\t" \
  "v_add_f32_e32 v235, v235, v99\n\t" \
  "v_cvt_pk_bf16_f32 v97, v98, v99\n\t" \
  "v_exp_f32_e32 v101, v101\n\t" \
  "v_mfma_f32_32x32x16_bf16 a[32:47], v[180:183], v[72:75], a[32:47]\n\t" \
  "v_add_f32_e32 v235, v235, v100\n\t" \
  "v_exp_f32_e32 v102, v102\n\t" \
  "v_add_f32_e32 v235, v235, v101\n\t" \
  "v_cvt_pk_bf16_f32 v98, v100, v101\n\t" \
  "v_exp_f32_e32 v103, v103\n\t" \
  "v_mfma_f32_32x32x16_bf16 a[48:63], v[180:183], v[88:91], a[48:63]\n\t" \
  "v_add_f32_e32 v235, v235, v102\n\t" \
  "v_exp_f32_e32 v112, v112\n\t" \
  "v_add_f32_e32 v235, v235, v103\n\t" \
  "v_cvt_pk_bf16_f32 v99, v102, v103\n\t" \
  "v_exp_f32_e32 v113, v113\n\t" \
  "v_mfma_f32_32x32x16_bf16 a[64:79], v[184:187], v[72:75], a[64:79]\n\t" \
  "v_add_f32_e32 v236, v236, v112\n\t" \
  "v_exp_f32_e32 v114, v114\n\t" \
  "v_add_f32_e32 v236, v236, v113\n\t" \
  "v_cvt_pk_bf16_f32 v112, v112, v113\n\t" \
  "v_exp_f32_e32 v115, v115\n\t" \
  "v_mfma_f32_32x32x16_bf16 a[80:95], v[184:187], v[88:91], a[80:95]\n\t" \
  "v_add_f32_e32 v236, v236, v114\n\t" \
  "v_exp_f32_e32 v116, v116\n\t" \
  "v_add_f32_e32 v236, v236, v115\n\t" \
  "v_cvt_pk_bf16_f32 v113, v114, v115\n\t" \
  "v_exp_f32_e32 v117, v117\n\t" \
  "v_mfma_f32_32x32x16_bf16 a[96:111], v[188:191], v[72:75], a[96:111]\n\t" \
  "v_add_f32_e32 v236, v236, v116\n\t" \
  "v_exp_f32_e32 v118, v118\n\t" \
  "v_add_f32_e32 v236, v236, v117\n\t" \
  "v_cvt_pk_bf16_f32 v114, v116, v117\n\t" \
  "v_exp_f32_e32 v119, v119\n\t" \
  "v_mfma_f32_32x32x16_bf16 a[112:127], v[188:191], v[88:91], a[112:127]\n\t" \
  "v_add_f32_e32 v236, v236, v118\n\t" \
  "v_exp_f32_e32 v104, v104\n\t" \
  "v_add_f32_e32 v236, v236, v119\n\t" \
  "v_cvt_pk_bf16_f32 v115, v118, v119\n\t" \
  "v_exp_f32_e32 v105, v105\n\t" \
  "s_nop 1\n\t" \
  "v_mfma_f32_32x32x16_bf16 a[0:15], v[192:195], v[96:99], a[0:15]\n\t" \
  "v_add_f32_e32 v235, v235, v104\n\t" \
  "v_exp_f32_e32 v106, v106\n\t" \
  "v_add_f32_e32 v235, v235, v105\n\t" \
  "v_cvt_pk_bf16_f32 v104, v104, v105\n\t" \
  "v_exp_f32_e32 v107, v107\n\t" \
  "v_mfma_f32_32x32x16_bf16 a[16:31], v[192:195], v[112:115], a[16:31]\n\t" \
  "v_add_f32_e32 v235, v235, v106\n\t" \
  "v_exp_f32_e32 v108, v108\n\t" \
  "v_add_f32_e32 v235, v235, v107\n\t" \
  "v_cvt_pk_bf16_f32 v105, v106, v107\n\t" \
  "v_exp_f32_e32 v109, v109\n\t" \
  "v_mfma_f32_32x32x16_bf16 a[32:47], v[196:199], v[96:99], a[32:47]\n\t" \
  "v_add_f32_e32 v235, v235, v108\n\t" \
  "v_exp_f32_e32 v110, v110\n\t" \
  "v_add_f32_e32 v235, v235, v109\n\t" \
  "v_cvt_pk_bf16_f32 v106, v108, v109\n\t" \
  "v_exp_f32_e32 v111, v111\n\t" \
  "v_mfma_f32_32x32x16_bf16 a[48:63], v[196:199], v[112:115], a[48:63]\n\t" \
  "v_add_f32_e32 v235, v235, v110\n\t" \
  "v_exp_f32_e32 v120, v120\n\t" \
  "v_add_f32_e32 v235, v235, v111\n\t" \
  "v_cvt_pk_bf16_f32 v107, v110, v111\n\t" \
  "v_exp_f32_e32 v121, v121\n\t" \
  "v_mfma_f32_32x32x16_bf16 a[64:79], v[200:203], v[96:99], a[64:79]\n\t" \
  "v_add_f32_e32 v236, v236, v120\n\t" \
  "v_exp_f32_e32 v122, v122\n\t" \
  "v_add_f32_e32 v236, v236, v121\n\t" \
  "v_cvt_pk_bf16_f32 v120, v120, v121\n\t" \
  "v_exp_f32_e32 v123, v123\n\t" \
  "v_mfma_f32_32x32x16_bf16 a[80:95], v[200:203], v[112:115], a[80:95]\n\t" \
  "v_add_f32_e32 v236, v236, v122\n\t" \
  "v_exp_f32_e32 v124, v124\n\t" \
  "v_add_f32_e32 v236, v236, v123\n\t" \
  "v_cvt_pk_bf16_f32 v121, v122, v123\n\t" \
  "v_exp_f32_e32 v125, v125\n\t" \
  "v_mfma_f32_32x32x16_bf16 a[96:111], v[204:207], v[96:99], a[96:111]\n\t" \
  "v_add_f32_e32 v236, v236, v124\n\t" \
  "v_exp_f32_e32 v126, v126\n\t" \
  "v_add_f32_e32 v236, v236, v125\n\t" \
  "v_cvt_pk_bf16_f32 v122, v124, v125\n\t" \
  "v_exp_f32_e32 v127, v127\n\t" \
  "v_mfma_f32_32x32x16_bf16 a[112:127], v[204:207], v[112:115], a[112:127]\n\t" \
  "v_add_f32_e32 v236, v236, v126\n\t" \
  "s_nop 0\n\t" \
  "v_add_f32_e32 v236, v236, v127\n\t" \
  "v_cvt_pk_bf16_f32 v123, v126, v127\n\t" \
  "s_nop 1\n\t" \
  "v_mfma_f32_32x32x16_bf16 a[0:15], v[208:211], v[104:107], a[0:15]\n\t" \
  "v_mfma_f32_32x32x16_bf16 a[16:31], v[208:211], v[120:123], a[16:31]\n\t" \
  "v_mfma_f32_32x32x16_bf16 a[32:47], v[212:215], v[104:107], a[32:47]\n\t" \
  "v_mfma_f32_32x32x16_bf16 a[48:63], v[212:215], v[120:123], a[48:63]\n\t" \
  "v_mfma_f32_32x32x16_bf16 a[64:79], v[216:219], v[104:107], a[64:79]\n\t" \
  "v_mfma_f32_32x32x16_bf16 a[80:95], v[216:219], v[120:123], a[80:95]\n\t" \
  "v_mfma_f32_32x32x16_bf16 a[96:111], v[220:223], v[104:107], a[96:111]\n\t" \
  "v_mfma_f32_32x32x16_bf16 a[112:127], v[220:223], v[120:123], a[112:127]\n\t" \
  "v_add_f32_e32 v239, v239, v235\n\t" \
  "v_add_f32_e32 v240, v240, v236\n\t" \
  "v_mov_b32_e32 v243, v239\n\t" \
  "v_mov_b32_e32 v244, v240\n\t" \
  "s_nop 1\n\t" \
  "v_permlane32_swap_b32_e32 v239, v243\n\t" \
  "v_permlane32_swap_b32_e32 v240, v244\n\t" \
  "v_add_f32_e32 v239, v239, v243\n\t" \
  "v_add_f32_e32 v240, v240, v244\n\t" \
  "s_nop 11\n\t" \
  "s_branch LEND_%=\n\t" \
  "LTA1_%=:\n\t" \
  "s_call_b64 s[42:43], LRA1_%=\n\t" \
  "s_branch LBA1_%=\n\t" \
  "LTB1_%=:\n\t" \
  "s_call_b64 s[42:43], LRB_%=\n\t" \
  "s_branch LBB1_%=\n\t" \
  "LTA2_%=:\n\t" \
  "s_call_b64 s[42:43], LRA0_%=\n\t" \
  "s_branch LBA2_%=\n\t" \
  "LTB2_%=:\n\t" \
  "s_call_b64 s[42:43], LRB_%=\n\t" \
  "s_branch LBB2_%=\n\t" \
  "LRA0_%=:\n\t" \
  "v_max_f32_e32 v249, 0, v245\n\t" \
  "v_add_f32_e32 v237, v237, v249\n\t" \
  "v_sub_f32_e32 v250, 0, v249\n\t" \
  "v_exp_f32_e32 v247, v250\n\t" \
  "v_sub_f32_e32 v0, v0, v249\n\t" \
  "v_sub_f32_e32 v1, v1, v249\n\t" \
  "v_sub_f32_e32 v2, v2, v249\n\t" \
  "v_sub_f32_e32 v3, v3, v249\n\t" \
  "v_sub_f32_e32 v4, v4, v249\n\t" \
  "v_sub_f32_e32 v5, v5, v249\n\t" \
  "v_sub_f32_e32 v6, v6, v249\n\t" \
  "v_sub_f32_e32 v7, v7, v249\n\t" \
  "v_sub_f32_e32 v8, v8, v249\n\t" \
  "v_sub_f32_e32 v9, v9, v249\n\t" \
  "v_sub_f32_e32 v10, v10, v249\n\t" \
  "v_sub_f32_e32 v11, v11, v249\n\t" \
  "v_sub_f32_e32 v12, v12, v249\n\t" \
  "v_sub_f32_e32 v13, v13, v249\n\t" \
  "v_sub_f32_e32 v14, v14, v249\n\t" \
  "v_sub_f32_e32 v15, v15, v249\n\t" \
  "v_sub_f32_e32 v32, v32, v249\n\t" \
  "v_sub_f32_e32 v33, v33, v249\n\t" \
  "v_sub_f32_e32 v34, v34, v249\n\t" \
  "v_sub_f32_e32 v35, v35, v249\n\t" \
  "v_sub_f32_e32 v36, v36, v249\n\t" \
  "v_sub_f32_e32 v37, v37, v249\n\t" \
  "v_sub_f32_e32 v38, v38, v249\n\t" \
  "v_sub_f32_e32 v39, v39, v249\n\t" \
  "v_sub_f32_e32 v40, v40, v249\n\t" \
  "v_sub_f32_e32 v41, v41, v249\n\t" \
  "v_sub_f32_e32 v42, v42, v249\n\t" \
  "v_sub_f32_e32 v43, v43, v249\n\t" \
  "v_sub_f32_e32 v44, v44, v249\n\t" \
  "v_sub_f32_e32 v45, v45, v249\n\t" \
  "v_sub_f32_e32 v46, v46, v249\n\t" \
  "v_sub_f32_e32 v47, v47, v249\n\t" \
  "v_sub_f32_e32 v128, 0, v237\n\t" \
  "v_mov_b32_e32 v129, v128\n\t" \
  "v_mov_b32_e32 v130, v128\n\t" \
  "v_mov_b32_e32 v131, v128\n\t" \
  "v_mov_b32_e32 v132, v128\n\t" \
  "v_mov_b32_e32 v133, v128\n\t" \
  "v_mov_b32_e32 v134, v128\n\t" \
  "v_mov_b32_e32 v135, v128\n\t" \
  "v_mov_b32_e32 v136, v128\n\t" \
  "v_mov_b32_e32 v137, v128\n\t" \
  "v_mov_b32_e32 v138, v128\n\t" \
  "v_mov_b32_e32 v139, v128\n\t" \
  "v_mov_b32_e32 v140, v128\n\t" \
  "v_mov_b32_e32 v141, v128\n\t" \
  "v_mov_b32_e32 v142, v128\n\t" \
  "v_mov_b32_e32 v143, v128\n\t" \
  "v_max_f32_e32 v249, 0, v246\n\t" \
  "v_add_f32_e32 v238, v238, v249\n\t" \
  "v_sub_f32_e32 v250, 0, v249\n\t" \
  "v_exp_f32_e32 v248, v250\n\t" \
  "v_sub_f32_e32 v16, v16, v249\n\t" \
  "v_sub_f32_e32 v17, v17, v249\n\t" \
  "v_sub_f32_e32 v18, v18, v249\n\t" \
  "v_sub_f32_e32 v19, v19, v249\n\t" \
  "v_sub_f32_e32 v20, v20, v249\n\t" \
  "v_sub_f32_e32 v21, v21, v249\n\t" \
  "v_sub_f32_e32 v22, v22, v249\n\t" \
  "v_sub_f32_e32 v23, v23, v249\n\t" \
  "v_sub_f32_e32 v24, v24, v249\n\t" \
  "v_sub_f32_e32 v25, v25, v249\n\t" \
  "v_sub_f32_e32 v26, v26, v249\n\t" \
  "v_sub_f32_e32 v27, v27, v249\n\t" \
  "v_sub_f32_e32 v28, v28, v249\n\t" \
  "v_sub_f32_e32 v29, v29, v249\n\t" \
  "v_sub_f32_e32 v30, v30, v249\n\t" \
  "v_sub_f32_e32 v31, v31, v249\n\t" \
  "v_sub_f32_e32 v48, v48, v249\n\t" \
  "v_sub_f32_e32 v49, v49, v249\n\t" \
  "v_sub_f32_e32 v50, v50, v249\n\t" \
  "v_sub_f32_e32 v51, v51, v249\n\t" \
  "v_sub_f32_e32 v52, v52, v249\n\t" \
  "v_sub_f32_e32 v53, v53, v249\n\t" \
  "v_sub_f32_e32 v54, v54, v249\n\t" \
  "v_sub_f32_e32 v55, v55, v249\n\t" \
  "v_sub_f32_e32 v56, v56, v249\n\t" \
  "v_sub_f32_e32 v57, v57, v249\n\t" \
  "v_sub_f32_e32 v58, v58, v249\n\t" \
  "v_sub_f32_e32 v59, v59, v249\n\t" \
  "v_sub_f32_e32 v60, v60, v249\n\t" \
  "v_sub_f32_e32 v61, v61, v249\n\t" \
  "v_sub_f32_e32 v62, v62, v249\n\t" \
  "v_sub_f32_e32 v63, v63, v249\n\t" \
  "v_sub_f32_e32 v144, 0, v238\n\t" \
  "v_mov_b32_e32 v145, v144\n\t" \
  "v_mov_b32_e32 v146, v144\n\t" \
  "v_mov_b32_e32 v147, v144\n\t" \
  "v_mov_b32_e32 v148, v144\n\t" \
  "v_mov_b32_e32 v149, v144\n\t" \
  "v_mov_b32_e32 v150, v144\n\t" \
  "v_mov_b32_e32 v151, v144\n\t" \
  "v_mov_b32_e32 v152, v144\n\t" \
  "v_mov_b32_e32 v153, v144\n\t" \
  "v_mov_b32_e32 v154, v144\n\t" \
  "v_mov_b32_e32 v155, v144\n\t" \
  "v_mov_b32_e32 v156, v144\n\t" \
  "v_mov_b32_e32 v157, v144\n\t" \
  "v_mov_b32_e32 v158, v144\n\t" \
  "v_mov_b32_e32 v159, v144\n\t" \
  "s_mov_b32 s40, 1\n\t" \
  "s_nop 1\n\t" \
  "s_setpc_b64 s[42:43]\n\t" \
  "LRA1_%=:\n\t" \
  "v_max_f32_e32 v249, 0, v245\n\t" \
  "v_add_f32_e32 v237, v237, v249\n\t" \
  "v_sub_f32_e32 v250, 0, v249\n\t" \
  "v_exp_f32_e32 v247, v250\n\t" \
  "v_sub_f32_e32 v64, v64, v249\n\t" \
  "v_sub_f32_e32 v65, v65, v249\n\t" \
  "v_sub_f32_e32 v66, v66, v249\n\t" \
  "v_sub_f32_e32 v67, v67, v249\n\t" \
  "v_sub_f32_e32 v68, v68, v249\n\t" \
  "v_sub_f32_e32 v69, v69, v249\n\t" \
  "v_sub_f32_e32 v70, v70, v249\n\t" \
  "v_sub_f32_e32 v71, v71, v249\n\t" \
  "v_sub_f32_e32 v72, v72, v249\n\t" \
  "v_sub_f32_e32 v73, v73, v249\n\t" \
  "v_sub_f32_e32 v74, v74, v249\n\t" \
  "v_sub_f32_e32 v75, v75, v249\n\t" \
  "v_sub_f32_e32 v76, v76, v249\n\t" \
  "v_sub_f32_e32 v77, v77, v249\n\t" \
  "v_sub_f32_e32 v78, v78, v249\n\t" \
  "v_sub_f32_e32 v79, v79, v249\n\t" \
  "v_sub_f32_e32 v96, v96, v249\n\t" \
  "v_sub_f32_e32 v97, v97, v249\n\t" \
  "v_sub_f32_e32 v98, v98, v249\n\t" \
  "v_sub_f32_e32 v99, v99, v249\n\t" \
  "v_sub_f32_e32 v100, v100, v249\n\t" \
  "v_sub_f32_e32 v101, v101, v249\n\t" \
  "v_sub_f32_e32 v102, v102, v249\n\t" \
  "v_sub_f32_e32 v103, v103, v249\n\t" \
  "v_sub_f32_e32 v104, v104, v249\n\t" \
  "v_sub_f32_e32 v105, v105, v249\n\t" \
  "v_sub_f32_e32 v106, v106, v249\n\t" \
  "v_sub_f32_e32 v107, v107, v249\n\t" \
  "v_sub_f32_e32 v108, v108, v249\n\t" \
  "v_sub_f32_e32 v109, v109, v249\n\t" \
  "v_sub_f32_e32 v110, v110, v249\n\t" \
  "v_sub_f32_e32 v111, v111, v249\n\t" \
  "v_sub_f32_e32 v128, 0, v237\n\t" \
  "v_mov_b32_e32 v129, v128\n\t" \
  "v_mov_b32_e32 v130, v128\n\t" \
  "v_mov_b32_e32 v131, v128\n\t" \
  "v_mov_b32_e32 v132, v128\n\t" \
  "v_mov_b32_e32 v133, v128\n\t" \
  "v_mov_b32_e32 v134, v128\n\t" \
  "v_mov_b32_e32 v135, v128\n\t" \
  "v_mov_b32_e32 v136, v128\n\t" \
  "v_mov_b32_e32 v137, v128\n\t" \
  "v_mov_b32_e32 v138, v128\n\t" \
  "v_mov_b32_e32 v139, v128\n\t" \
  "v_mov_b32_e32 v140, v128\n\t" \
  "v_mov_b32_e32 v141, v128\n\t" \
  "v_mov_b32_e32 v142, v128\n\t" \
  "v_mov_b32_e32 v143, v128\n\t" \
  "v_max_f32_e32 v249, 0, v246\n\t" \
  "v_add_f32_e32 v238, v238, v249\n\t" \
  "v_sub_f32_e32 v250, 0, v249\n\t" \
  "v_exp_f32_e32 v248, v250\n\t" \
  "v_sub_f32_e32 v80, v80, v249\n\t" \
  "v_sub_f32_e32 v81, v81, v249\n\t" \
  "v_sub_f32_e32 v82, v82, v249\n\t" \
  "v_sub_f32_e32 v83, v83, v249\n\t" \
  "v_sub_f32_e32 v84, v84, v249\n\t" \
  "v_sub_f32_e32 v85, v85, v249\n\t" \
  "v_sub_f32_e32 v86, v86, v249\n\t" \
  "v_sub_f32_e32 v87, v87, v249\n\t" \
  "v_sub_f32_e32 v88, v88, v249\n\t" \
  "v_sub_f32_e32 v89, v89, v249\n\t" \
  "v_sub_f32_e32 v90, v90, v249\n\t" \
  "v_sub_f32_e32 v91, v91, v249\n\t" \
  "v_sub_f32_e32 v92, v92, v249\n\t" \
  "v_sub_f32_e32 v93, v93, v249\n\t" \
  "v_sub_f32_e32 v94, v94, v249\n\t" \
  "v_sub_f32_e32 v95, v95, v249\n\t" \
  "v_sub_f32_e32 v112, v112, v249\n\t" \
  "v_sub_f32_e32 v113, v113, v249\n\t" \
  "v_sub_f32_e32 v114, v114, v249\n\t" \
  "v_sub_f32_e32 v115, v115, v249\n\t" \
  "v_sub_f32_e32 v116, v116, v249\n\t" \
  "v_sub_f32_e32 v117, v117, v249\n\t" \
  "v_sub_f32_e32 v118, v118, v249\n\t" \
  "v_sub_f32_e32 v119, v119, v249\n\t" \
  "v_sub_f32_e32 v120, v120, v249\n\t" \
  "v_sub_f32_e32 v121, v121, v249\n\t" \
  "v_sub_f32_e32 v122, v122, v249\n\t" \
  "v_sub_f32_e32 v123, v123, v249\n\t" \
  "v_sub_f32_e32 v124, v124, v249\n\t" \
  "v_sub_f32_e32 v125, v125, v249\n\t" \
  "v_sub_f32_e32 v126, v126, v249\n\t" \
  "v_sub_f32_e32 v127, v127, v249\n\t" \
  "v_sub_f32_e32 v144, 0, v238\n\t" \
  "v_mov_b32_e32 v145, v144\n\t" \
  "v_mov_b32_e32 v146, v144\n\t" \
  "v_mov_b32_e32 v147, v144\n\t" \
  "v_mov_b32_e32 v148, v144\n\t" \
  "v_mov_b32_e32 v149, v144\n\t" \
  "v_mov_b32_e32 v150, v144\n\t" \
  "v_mov_b32_e32 v151, v144\n\t" \
  "v_mov_b32_e32 v152, v144\n\t" \
  "v_mov_b32_e32 v153, v144\n\t" \
  "v_mov_b32_e32 v154, v144\n\t" \
  "v_mov_b32_e32 v155, v144\n\t" \
  "v_mov_b32_e32 v156, v144\n\t" \
  "v_mov_b32_e32 v157, v144\n\t" \
  "v_mov_b32_e32 v158, v144\n\t" \
  "v_mov_b32_e32 v159, v144\n\t" \
  "s_mov_b32 s40, 1\n\t" \
  "s_nop 1\n\t" \
  "s_setpc_b64 s[42:43]\n\t" \
  "LRB_%=:\n\t" \
  "s_nop 7\n\t" \
  "s_nop 7\n\t" \
  "v_accvgpr_read_b32 v250, a0\n\t" \
  "v_mul_f32_e32 v250, v247, v250\n\t" \
  "v_accvgpr_write_b32 a0, v250\n\t" \
  "v_accvgpr_read_b32 v249, a1\n\t" \
  "v_mul_f32_e32 v249, v247, v249\n\t" \
  "v_accvgpr_write_b32 a1, v249\n\t" \
  "v_accvgpr_read_b32 v250, a2\n\t" \
  "v_mul_f32_e32 v250, v247, v250\n\t" \
  "v_accvgpr_write_b32 a2, v250\n\t" \
  "v_accvgpr_read_b32 v249, a3\n\t" \
  "v_mul_f32_e32 v249, v247, v249\n\t" \
  "v_accvgpr_write_b32 a3, v249\n\t" \
  "v_accvgpr_read_b32 v250, a4\n\t" \
  "v_mul_f32_e32 v250, v247, v250\n\t" \
  "v_accvgpr_write_b32 a4, v250\n\t" \
  "v_accvgpr_read_b32 v249, a5\n\t" \
  "v_mul_f32_e32 v249, v247, v249\n\t" \
  "v_accvgpr_write_b32 a5, v249\n\t" \
  "v_accvgpr_read_b32 v250, a6\n\t" \
  "v_mul_f32_e32 v250, v247, v250\n\t" \
  "v_accvgpr_write_b32 a6, v250\n\t" \
  "v_accvgpr_read_b32 v249, a7\n\t" \
  "v_mul_f32_e32 v249, v247, v249\n\t" \
  "v_accvgpr_write_b32 a7, v249\n\t" \
  "v_accvgpr_read_b32 v250, a8\n\t" \
  "v_mul_f32_e32 v250, v247, v250\n\t" \
  "v_accvgpr_write_b32 a8, v250\n\t" \
  "v_accvgpr_read_b32 v249, a9\n\t" \
  "v_mul_f32_e32 v249, v247, v249\n\t" \
  "v_accvgpr_write_b32 a9, v249\n\t" \
  "v_accvgpr_read_b32 v250, a10\n\t" \
  "v_mul_f32_e32 v250, v247, v250\n\t" \
  "v_accvgpr_write_b32 a10, v250\n\t" \
  "v_accvgpr_read_b32 v249, a11\n\t" \
  "v_mul_f32_e32 v249, v247, v249\n\t" \
  "v_accvgpr_write_b32 a11, v249\n\t" \
  "v_accvgpr_read_b32 v250, a12\n\t" \
  "v_mul_f32_e32 v250, v247, v250\n\t" \
  "v_accvgpr_write_b32 a12, v250\n\t" \
  "v_accvgpr_read_b32 v249, a13\n\t" \
  "v_mul_f32_e32 v249, v247, v249\n\t" \
  "v_accvgpr_write_b32 a13, v249\n\t" \
  "v_accvgpr_read_b32 v250, a14\n\t" \
  "v_mul_f32_e32 v250, v247, v250\n\t" \
  "v_accvgpr_write_b32 a14, v250\n\t" \
  "v_accvgpr_read_b32 v249, a15\n\t" \
  "v_mul_f32_e32 v249, v247, v249\n\t" \
  "v_accvgpr_write_b32 a15, v249\n\t" \
  "v_accvgpr_read_b32 v250, a16\n\t" \
  "v_mul_f32_e32 v250, v248, v250\n\t" \
  "v_accvgpr_write_b32 a16, v250\n\t" \
  "v_accvgpr_read_b32 v249, a17\n\t" \
  "v_mul_f32_e32 v249, v248, v249\n\t" \
  "v_accvgpr_write_b32 a17, v249\n\t" \
  "v_accvgpr_read_b32 v250, a18\n\t" \
  "v_mul_f32_e32 v250, v248, v250\n\t" \
  "v_accvgpr_write_b32 a18, v250\n\t" \
  "v_accvgpr_read_b32 v249, a19\n\t" \
  "v_mul_f32_e32 v249, v248, v249\n\t" \
  "v_accvgpr_write_b32 a19, v249\n\t" \
  "v_accvgpr_read_b32 v250, a20\n\t" \
  "v_mul_f32_e32 v250, v248, v250\n\t" \
  "v_accvgpr_write_b32 a20, v250\n\t" \
  "v_accvgpr_read_b32 v249, a21\n\t" \
  "v_mul_f32_e32 v249, v248, v249\n\t" \
  "v_accvgpr_write_b32 a21, v249\n\t" \
  "v_accvgpr_read_b32 v250, a22\n\t" \
  "v_mul_f32_e32 v250, v248, v250\n\t" \
  "v_accvgpr_write_b32 a22, v250\n\t" \
  "v_accvgpr_read_b32 v249, a23\n\t" \
  "v_mul_f32_e32 v249, v248, v249\n\t" \
  "v_accvgpr_write_b32 a23, v249\n\t" \
  "v_accvgpr_read_b32 v250, a24\n\t" \
  "v_mul_f32_e32 v250, v248, v250\n\t" \
  "v_accvgpr_write_b32 a24, v250\n\t" \
  "v_accvgpr_read_b32 v249, a25\n\t" \
  "v_mul_f32_e32 v249, v248, v249\n\t" \
  "v_accvgpr_write_b32 a25, v249\n\t" \
  "v_accvgpr_read_b32 v250, a26\n\t" \
  "v_mul_f32_e32 v250, v248, v250\n\t" \
  "v_accvgpr_write_b32 a26, v250\n\t" \
  "v_accvgpr_read_b32 v249, a27\n\t" \
  "v_mul_f32_e32 v249, v248, v249\n\t" \
  "v_accvgpr_write_b32 a27, v249\n\t" \
  "v_accvgpr_read_b32 v250, a28\n\t" \
  "v_mul_f32_e32 v250, v248, v250\n\t" \
  "v_accvgpr_write_b32 a28, v250\n\t" \
  "v_accvgpr_read_b32 v249, a29\n\t" \
  "v_mul_f32_e32 v249, v248, v249\n\t" \
  "v_accvgpr_write_b32 a29, v249\n\t" \
  "v_accvgpr_read_b32 v250, a30\n\t" \
  "v_mul_f32_e32 v250, v248, v250\n\t" \
  "v_accvgpr_write_b32 a30, v250\n\t" \
  "v_accvgpr_read_b32 v249, a31\n\t" \
  "v_mul_f32_e32 v249, v248, v249\n\t" \
  "v_accvgpr_write_b32 a31, v249\n\t" \
  "v_accvgpr_read_b32 v250, a32\n\t" \
  "v_mul_f32_e32 v250, v247, v250\n\t" \
  "v_accvgpr_write_b32 a32, v250\n\t" \
  "v_accvgpr_read_b32 v249, a33\n\t" \
  "v_mul_f32_e32 v249, v247, v249\n\t" \
  "v_accvgpr_write_b32 a33, v249\n\t" \
  "v_accvgpr_read_b32 v250, a34\n\t" \
  "v_mul_f32_e32 v250, v247, v250\n\t" \
  "v_accvgpr_write_b32 a34, v250\n\t" \
  "v_accvgpr_read_b32 v249, a35\n\t" \
  "v_mul_f32_e32 v249, v247, v249\n\t" \
  "v_accvgpr_write_b32 a35, v249\n\t" \
  "v_accvgpr_read_b32 v250, a36\n\t" \
  "v_mul_f32_e32 v250, v247, v250\n\t" \
  "v_accvgpr_write_b32 a36, v250\n\t" \
  "v_accvgpr_read_b32 v249, a37\n\t" \
  "v_mul_f32_e32 v249, v247, v249\n\t" \
  "v_accvgpr_write_b32 a37, v249\n\t" \
  "v_accvgpr_read_b32 v250, a38\n\t" \
  "v_mul_f32_e32 v250, v247, v250\n\t" \
  "v_accvgpr_write_b32 a38, v250\n\t" \
  "v_accvgpr_read_b32 v249, a39\n\t" \
  "v_mul_f32_e32 v249, v247, v249\n\t" \
  "v_accvgpr_write_b32 a39, v249\n\t" \
  "v_accvgpr_read_b32 v250, a40\n\t" \
  "v_mul_f32_e32 v250, v247, v250\n\t" \
  "v_accvgpr_write_b32 a40, v250\n\t" \
  "v_accvgpr_read_b32 v249, a41\n\t" \
  "v_mul_f32_e32 v249, v247, v249\n\t" \
  "v_accvgpr_write_b32 a41, v249\n\t" \
  "v_accvgpr_read_b32 v250, a42\n\t" \
  "v_mul_f32_e32 v250, v247, v250\n\t" \
  "v_accvgpr_write_b32 a42, v250\n\t" \
  "v_accvgpr_read_b32 v249, a43\n\t" \
  "v_mul_f32_e32 v249, v247, v249\n\t" \
  "v_accvgpr_write_b32 a43, v249\n\t" \
  "v_accvgpr_read_b32 v250, a44\n\t" \
  "v_mul_f32_e32 v250, v247, v250\n\t" \
  "v_accvgpr_write_b32 a44, v250\n\t" \
  "v_accvgpr_read_b32 v249, a45\n\t" \
  "v_mul_f32_e32 v249, v247, v249\n\t" \
  "v_accvgpr_write_b32 a45, v249\n\t" \
  "v_accvgpr_read_b32 v250, a46\n\t" \
  "v_mul_f32_e32 v250, v247, v250\n\t" \
  "v_accvgpr_write_b32 a46, v250\n\t" \
  "v_accvgpr_read_b32 v249, a47\n\t" \
  "v_mul_f32_e32 v249, v247, v249\n\t" \
  "v_accvgpr_write_b32 a47, v249\n\t" \
  "v_accvgpr_read_b32 v250, a48\n\t" \
  "v_mul_f32_e32 v250, v248, v250\n\t" \
  "v_accvgpr_write_b32 a48, v250\n\t" \
  "v_accvgpr_read_b32 v249, a49\n\t" \
  "v_mul_f32_e32 v249, v248, v249\n\t" \
  "v_accvgpr_write_b32 a49, v249\n\t" \
  "v_accvgpr_read_b32 v250, a50\n\t" \
  "v_mul_f32_e32 v250, v248, v250\n\t" \
  "v_accvgpr_write_b32 a50, v250\n\t" \
  "v_accvgpr_read_b32 v249, a51\n\t" \
  "v_mul_f32_e32 v249, v248, v249\n\t" \
  "v_accvgpr_write_b32 a51, v249\n\t" \
  "v_accvgpr_read_b32 v250, a52\n\t" \
  "v_mul_f32_e32 v250, v248, v250\n\t" \
  "v_accvgpr_write_b32 a52, v250\n\t" \
  "v_accvgpr_read_b32 v249, a53\n\t" \
  "v_mul_f32_e32 v249, v248, v249\n\t" \
  "v_accvgpr_write_b32 a53, v249\n\t" \
  "v_accvgpr_read_b32 v250, a54\n\t" \
  "v_mul_f32_e32 v250, v248, v250\n\t" \
  "v_accvgpr_write_b32 a54, v250\n\t" \
  "v_accvgpr_read_b32 v249, a55\n\t" \
  "v_mul_f32_e32 v249, v248, v249\n\t" \
  "v_accvgpr_write_b32 a55, v249\n\t" \
  "v_accvgpr_read_b32 v250, a56\n\t" \
  "v_mul_f32_e32 v250, v248, v250\n\t" \
  "v_accvgpr_write_b32 a56, v250\n\t" \
  "v_accvgpr_read_b32 v249, a57\n\t" \
  "v_mul_f32_e32 v249, v248, v249\n\t" \
  "v_accvgpr_write_b32 a57, v249\n\t" \
  "v_accvgpr_read_b32 v250, a58\n\t" \
  "v_mul_f32_e32 v250, v248, v250\n\t" \
  "v_accvgpr_write_b32 a58, v250\n\t" \
  "v_accvgpr_read_b32 v249, a59\n\t" \
  "v_mul_f32_e32 v249, v248, v249\n\t" \
  "v_accvgpr_write_b32 a59, v249\n\t" \
  "v_accvgpr_read_b32 v250, a60\n\t" \
  "v_mul_f32_e32 v250, v248, v250\n\t" \
  "v_accvgpr_write_b32 a60, v250\n\t" \
  "v_accvgpr_read_b32 v249, a61\n\t" \
  "v_mul_f32_e32 v249, v248, v249\n\t" \
  "v_accvgpr_write_b32 a61, v249\n\t" \
  "v_accvgpr_read_b32 v250, a62\n\t" \
  "v_mul_f32_e32 v250, v248, v250\n\t" \
  "v_accvgpr_write_b32 a62, v250\n\t" \
  "v_accvgpr_read_b32 v249, a63\n\t" \
  "v_mul_f32_e32 v249, v248, v249\n\t" \
  "v_accvgpr_write_b32 a63, v249\n\t" \
  "v_accvgpr_read_b32 v250, a64\n\t" \
  "v_mul_f32_e32 v250, v247, v250\n\t" \
  "v_accvgpr_write_b32 a64, v250\n\t" \
  "v_accvgpr_read_b32 v249, a65\n\t" \
  "v_mul_f32_e32 v249, v247, v249\n\t" \
  "v_accvgpr_write_b32 a65, v249\n\t" \
  "v_accvgpr_read_b32 v250, a66\n\t" \
  "v_mul_f32_e32 v250, v247, v250\n\t" \
  "v_accvgpr_write_b32 a66, v250\n\t" \
  "v_accvgpr_read_b32 v249, a67\n\t" \
  "v_mul_f32_e32 v249, v247, v249\n\t" \
  "v_accvgpr_write_b32 a67, v249\n\t" \
  "v_accvgpr_read_b32 v250, a68\n\t" \
  "v_mul_f32_e32 v250, v247, v250\n\t" \
  "v_accvgpr_write_b32 a68, v250\n\t" \
  "v_accvgpr_read_b32 v249, a69\n\t" \
  "v_mul_f32_e32 v249, v247, v249\n\t" \
  "v_accvgpr_write_b32 a69, v249\n\t" \
  "v_accvgpr_read_b32 v250, a70\n\t" \
  "v_mul_f32_e32 v250, v247, v250\n\t" \
  "v_accvgpr_write_b32 a70, v250\n\t" \
  "v_accvgpr_read_b32 v249, a71\n\t" \
  "v_mul_f32_e32 v249, v247, v249\n\t" \
  "v_accvgpr_write_b32 a71, v249\n\t" \
  "v_accvgpr_read_b32 v250, a72\n\t" \
  "v_mul_f32_e32 v250, v247, v250\n\t" \
  "v_accvgpr_write_b32 a72, v250\n\t" \
  "v_accvgpr_read_b32 v249, a73\n\t" \
  "v_mul_f32_e32 v249, v247, v249\n\t" \
  "v_accvgpr_write_b32 a73, v249\n\t" \
  "v_accvgpr_read_b32 v250, a74\n\t" \
  "v_mul_f32_e32 v250, v247, v250\n\t" \
  "v_accvgpr_write_b32 a74, v250\n\t" \
  "v_accvgpr_read_b32 v249, a75\n\t" \
  "v_mul_f32_e32 v249, v247, v249\n\t" \
  "v_accvgpr_write_b32 a75, v249\n\t" \
  "v_accvgpr_read_b32 v250, a76\n\t" \
  "v_mul_f32_e32 v250, v247, v250\n\t" \
  "v_accvgpr_write_b32 a76, v250\n\t" \
  "v_accvgpr_read_b32 v249, a77\n\t" \
  "v_mul_f32_e32 v249, v247, v249\n\t" \
  "v_accvgpr_write_b32 a77, v249\n\t" \
  "v_accvgpr_read_b32 v250, a78\n\t" \
  "v_mul_f32_e32 v250, v247, v250\n\t" \
  "v_accvgpr_write_b32 a78, v250\n\t" \
  "v_accvgpr_read_b32 v249, a79\n\t" \
  "v_mul_f32_e32 v249, v247, v249\n\t" \
  "v_accvgpr_write_b32 a79, v249\n\t" \
  "v_accvgpr_read_b32 v250, a80\n\t" \
  "v_mul_f32_e32 v250, v248, v250\n\t" \
  "v_accvgpr_write_b32 a80, v250\n\t" \
  "v_accvgpr_read_b32 v249, a81\n\t" \
  "v_mul_f32_e32 v249, v248, v249\n\t" \
  "v_accvgpr_write_b32 a81, v249\n\t" \
  "v_accvgpr_read_b32 v250, a82\n\t" \
  "v_mul_f32_e32 v250, v248, v250\n\t" \
  "v_accvgpr_write_b32 a82, v250\n\t" \
  "v_accvgpr_read_b32 v249, a83\n\t" \
  "v_mul_f32_e32 v249, v248, v249\n\t" \
  "v_accvgpr_write_b32 a83, v249\n\t" \
  "v_accvgpr_read_b32 v250, a84\n\t" \
  "v_mul_f32_e32 v250, v248, v250\n\t" \
  "v_accvgpr_write_b32 a84, v250\n\t" \
  "v_accvgpr_read_b32 v249, a85\n\t" \
  "v_mul_f32_e32 v249, v248, v249\n\t" \
  "v_accvgpr_write_b32 a85, v249\n\t" \
  "v_accvgpr_read_b32 v250, a86\n\t" \
  "v_mul_f32_e32 v250, v248, v250\n\t" \
  "v_accvgpr_write_b32 a86, v250\n\t" \
  "v_accvgpr_read_b32 v249, a87\n\t" \
  "v_mul_f32_e32 v249, v248, v249\n\t" \
  "v_accvgpr_write_b32 a87, v249\n\t" \
  "v_accvgpr_read_b32 v250, a88\n\t" \
  "v_mul_f32_e32 v250, v248, v250\n\t" \
  "v_accvgpr_write_b32 a88, v250\n\t" \
  "v_accvgpr_read_b32 v249, a89\n\t" \
  "v_mul_f32_e32 v249, v248, v249\n\t" \
  "v_accvgpr_write_b32 a89, v249\n\t" \
  "v_accvgpr_read_b32 v250, a90\n\t" \
  "v_mul_f32_e32 v250, v248, v250\n\t" \
  "v_accvgpr_write_b32 a90, v250\n\t" \
  "v_accvgpr_read_b32 v249, a91\n\t" \
  "v_mul_f32_e32 v249, v248, v249\n\t" \
  "v_accvgpr_write_b32 a91, v249\n\t" \
  "v_accvgpr_read_b32 v250, a92\n\t" \
  "v_mul_f32_e32 v250, v248, v250\n\t" \
  "v_accvgpr_write_b32 a92, v250\n\t" \
  "v_accvgpr_read_b32 v249, a93\n\t" \
  "v_mul_f32_e32 v249, v248, v249\n\t" \
  "v_accvgpr_write_b32 a93, v249\n\t" \
  "v_accvgpr_read_b32 v250, a94\n\t" \
  "v_mul_f32_e32 v250, v248, v250\n\t" \
  "v_accvgpr_write_b32 a94, v250\n\t" \
  "v_accvgpr_read_b32 v249, a95\n\t" \
  "v_mul_f32_e32 v249, v248, v249\n\t" \
  "v_accvgpr_write_b32 a95, v249\n\t" \
  "v_accvgpr_read_b32 v250, a96\n\t" \
  "v_mul_f32_e32 v250, v247, v250\n\t" \
  "v_accvgpr_write_b32 a96, v250\n\t" \
  "v_accvgpr_read_b32 v249, a97\n\t" \
  "v_mul_f32_e32 v249, v247, v249\n\t" \
  "v_accvgpr_write_b32 a97, v249\n\t" \
  "v_accvgpr_read_b32 v250, a98\n\t" \
  "v_mul_f32_e32 v250, v247, v250\n\t" \
  "v_accvgpr_write_b32 a98, v250\n\t" \
  "v_accvgpr_read_b32 v249, a99\n\t" \
  "v_mul_f32_e32 v249, v247, v249\n\t" \
  "v_accvgpr_write_b32 a99, v249\n\t" \
  "v_accvgpr_read_b32 v250, a100\n\t" \
  "v_mul_f32_e32 v250, v247, v250\n\t" \
  "v_accvgpr_write_b32 a100, v250\n\t" \
  "v_accvgpr_read_b32 v249, a101\n\t" \
  "v_mul_f32_e32 v249, v247, v249\n\t" \
  "v_accvgpr_write_b32 a101, v249\n\t" \
  "v_accvgpr_read_b32 v250, a102\n\t" \
  "v_mul_f32_e32 v250, v247, v250\n\t" \
  "v_accvgpr_write_b32 a102, v250\n\t" \
  "v_accvgpr_read_b32 v249, a103\n\t" \
  "v_mul_f32_e32 v249, v247, v249\n\t" \
  "v_accvgpr_write_b32 a103, v249\n\t" \
  "v_accvgpr_read_b32 v250, a104\n\t" \
  "v_mul_f32_e32 v250, v247, v250\n\t" \
  "v_accvgpr_write_b32 a104, v250\n\t" \
  "v_accvgpr_read_b32 v249, a105\n\t" \
  "v_mul_f32_e32 v249, v247, v249\n\t" \
  "v_accvgpr_write_b32 a105, v249\n\t" \
  "v_accvgpr_read_b32 v250, a106\n\t" \
  "v_mul_f32_e32 v250, v247, v250\n\t" \
  "v_accvgpr_write_b32 a106, v250\n\t" \
  "v_accvgpr_read_b32 v249, a107\n\t" \
  "v_mul_f32_e32 v249, v247, v249\n\t" \
  "v_accvgpr_write_b32 a107, v249\n\t" \
  "v_accvgpr_read_b32 v250, a108\n\t" \
  "v_mul_f32_e32 v250, v247, v250\n\t" \
  "v_accvgpr_write_b32 a108, v250\n\t" \
  "v_accvgpr_read_b32 v249, a109\n\t" \
  "v_mul_f32_e32 v249, v247, v249\n\t" \
  "v_accvgpr_write_b32 a109, v249\n\t" \
  "v_accvgpr_read_b32 v250, a110\n\t" \
  "v_mul_f32_e32 v250, v247, v250\n\t" \
  "v_accvgpr_write_b32 a110, v250\n\t" \
  "v_accvgpr_read_b32 v249, a111\n\t" \
  "v_mul_f32_e32 v249, v247, v249\n\t" \
  "v_accvgpr_write_b32 a111, v249\n\t" \
  "v_accvgpr_read_b32 v250, a112\n\t" \
  "v_mul_f32_e32 v250, v248, v250\n\t" \
  "v_accvgpr_write_b32 a112, v250\n\t" \
  "v_accvgpr_read_b32 v249, a113\n\t" \
  "v_mul_f32_e32 v249, v248, v249\n\t" \
  "v_accvgpr_write_b32 a113, v249\n\t" \
  "v_accvgpr_read_b32 v250, a114\n\t" \
  "v_mul_f32_e32 v250, v248, v250\n\t" \
  "v_accvgpr_write_b32 a114, v250\n\t" \
  "v_accvgpr_read_b32 v249, a115\n\t" \
  "v_mul_f32_e32 v249, v248, v249\n\t" \
  "v_accvgpr_write_b32 a115, v249\n\t" \
  "v_accvgpr_read_b32 v250, a116\n\t" \
  "v_mul_f32_e32 v250, v248, v250\n\t" \
  "v_accvgpr_write_b32 a116, v250\n\t" \
  "v_accvgpr_read_b32 v249, a117\n\t" \
  "v_mul_f32_e32 v249, v248, v249\n\t" \
  "v_accvgpr_write_b32 a117, v249\n\t" \
  "v_accvgpr_read_b32 v250, a118\n\t" \
  "v_mul_f32_e32 v250, v248, v250\n\t" \
  "v_accvgpr_write_b32 a118, v250\n\t" \
  "v_accvgpr_read_b32 v249, a119\n\t" \
  "v_mul_f32_e32 v249, v248, v249\n\t" \
  "v_accvgpr_write_b32 a119, v249\n\t" \
  "v_accvgpr_read_b32 v250, a120\n\t" \
  "v_mul_f32_e32 v250, v248, v250\n\t" \
  "v_accvgpr_write_b32 a120, v250\n\t" \
  "v_accvgpr_read_b32 v249, a121\n\t" \
  "v_mul_f32_e32 v249, v248, v249\n\t" \
  "v_accvgpr_write_b32 a121, v249\n\t" \
  "v_accvgpr_read_b32 v250, a122\n\t" \
  "v_mul_f32_e32 v250, v248, v250\n\t" \
  "v_accvgpr_write_b32 a122, v250\n\t" \
  "v_accvgpr_read_b32 v249, a123\n\t" \
  "v_mul_f32_e32 v249, v248, v249\n\t" \
  "v_accvgpr_write_b32 a123, v249\n\t" \
  "v_accvgpr_read_b32 v250, a124\n\t" \
  "v_mul_f32_e32 v250, v248, v250\n\t" \
  "v_accvgpr_write_b32 a124, v250\n\t" \
  "v_accvgpr_read_b32 v249, a125\n\t" \
  "v_mul_f32_e32 v249, v248, v249\n\t" \
  "v_accvgpr_write_b32 a125, v249\n\t" \
  "v_accvgpr_read_b32 v250, a126\n\t" \
  "v_mul_f32_e32 v250, v248, v250\n\t" \
  "v_accvgpr_write_b32 a126, v250\n\t" \
  "v_accvgpr_read_b32 v249, a127\n\t" \
  "v_mul_f32_e32 v249, v248, v249\n\t" \
  "v_accvgpr_write_b32 a127, v249\n\t" \
  "v_mul_f32_e32 v239, v247, v239\n\t" \
  "v_mul_f32_e32 v240, v248, v240\n\t" \
  "s_mov_b32 s40, 0\n\t" \
  "s_nop 1\n\t" \
  "s_setpc_b64 s[42:43]\n\t" \
  "LEND_%=:\n\t" \
  ""
#define MAINLOOP_CLOB "v0","v1","v2","v3","v4","v5","v6","v7","v8","v9","v10","v11","v12","v13","v14","v15","v16","v17","v18","v19","v20","v21","v22","v23","v24","v25","v26","v27","v28","v29","v30","v31","v32","v33","v34","v35","v36","v37","v38","v39","v40","v41","v42","v43","v44","v45","v46","v47","v48","v49","v50","v51","v52","v53","v54","v55","v56","v57","v58","v59","v60","v61","v62","v63","v64","v65","v66","v67","v68","v69","v70","v71","v72","v73","v74","v75","v76","v77","v78","v79","v80","v81","v82","v83","v84","v85","v86","v87","v88","v89","v90","v91","v92","v93","v94","v95","v96","v97","v98","v99","v100","v101","v102","v103","v104","v105","v106","v107","v108","v109","v110","v111","v112","v113","v114","v115","v116","v117","v118","v119","v120","v121","v122","v123","v124","v125","v126","v127","v132","v135","v142","v143","v144","v145","v146","v147","v148","v149","v150","v151","v152","v153","v154","v155","v156","v157","v158","v159","v160","v161","v162","v163","v164","v165","v166","v167","v168","v169","v170","v171","v172","v173","v174","v175","v176","v177","v178","v179","v180","v181","v182","v183","v184","v185","v186","v187","v188","v189","v190","v191","v192","v193","v194","v195","v196","v197","v198","v199","v200","v201","v202","v203","v204","v205","v206","v207","v208","v209","v210","v211","v212","v213","v214","v215","v216","v217","v218","v219","v220","v221","v222","v223","v233","v234","v235","v236","v237","v238","v241","v242","v243","v244","v245","v246","v247","v248","v249","v250","a0","a1","a2","a3","a4","a5","a6","a7","a8","a9","a10","a11","a12","a13","a14","a15","a16","a17","a18","a19","a20","a21","a22","a23","a24","a25","a26","a27","a28","a29","a30","a31","a32","a33","a34","a35","a36","a37","a38","a39","a40","a41","a42","a43","a44","a45","a46","a47","a48","a49","a50","a51","a52","a53","a54","a55","a56","a57","a58","a59","a60","a61","a62","a63","a64","a65","a66","a67","a68","a69","a70","a71","a72","a73","a74","a75","a76","a77","a78","a79","a80","a81","a82","a83","a84","a85","a86","a87","a88","a89","a90","a91","a92","a93","a94","a95","a96","a97","a98","a99","a100","a101","a102","a103","a104","a105","a106","a107","a108","a109","a110","a111","a112","a113","a114","a115","a116","a117","a118","a119","a120","a121","a122","a123","a124","a125","a126","a127","a128","a129","a130","a131","a132","a133","a134","a135","a136","a137","a138","a139","a140","a141","a142","a143","a144","a145","a146","a147","a148","a149","a150","a151","a152","a153","a154","a155","a156","a157","a158","a159","a160","a161","a162","a163","a164","a165","a166","a167","a168","a169","a170","a171","a172","a173","a174","a175","a176","a177","a178","a179","a180","a181","a182","a183","a184","a185","a186","a187","a188","a189","a190","a191","a192","a193","a194","a195","a196","a197","a198","a199","a200","a201","a202","a203","a204","a205","a206","a207","a208","a209","a210","a211","a212","a213","a214","a215","a216","a217","a218","a219","a220","a221","a222","a223","a224","a225","a226","a227","a228","a229","a230","a231","a232","a233","a234","a235","a236","a237","a238","a239","a240","a241","a242","a243","a244","a245","a246","a247","a248","a249","a250","a251","a252","a253","a254","a255","s40","s41","s42","s43","s44","s45","s46","s47","vcc","scc","m0","memory"

#define XWPOL ""
#define OSTPOL "sc1"
#define XREC 65536
#define WREC (int)(512u * WRS - h * 256u)
#define KVREC (1024 * 256)
constexpr unsigned WRS = 8192u;
struct QPC { __amdgpu_buffer_rsrc_t srdW, srdX; };

extern "C" __global__ __launch_bounds__(256, 1)
void attn_fused(const unsigned short* __restrict__ X, const unsigned short* __restrict__ Kc, const unsigned short* __restrict__ Vc,
                const unsigned short* __restrict__ Wq, unsigned short* __restrict__ O, const unsigned short* __restrict__ Wo) {
  extern __shared__ __attribute__((aligned(16))) char lds[];
  const unsigned tid = threadIdx.x, lane = tid & 63u, wid = tid >> 6, l32 = lane & 31u, hi = lane >> 5;
  const float C = 0.12751743082f;
#define STAMP(i)
  STAMP(0);
  const unsigned bx = blockIdx.x, xcd = bx & 7u, i5 = bx >> 3;
  const unsigned b = (xcd & 1u) * 4u + (i5 & 3u), h = (xcd >> 1) * 8u + (i5 >> 2);
  W4C c; QPC p;
  c.ldsb = (unsigned)(size_t)lds;
  #pragma unroll
  for (int j = 0; j < 4; ++j) c.vkb[j] = c.ldsb + KRING + 256u * l32 + 16u * ((2u * j + hi) ^ (l32 & 7u));
  c.vvb = c.ldsb + VRING + (((lane & 3u) << 3) | (((lane >> 2) & 3u) << 6) | (((lane >> 4) & 1u) << 5) | (hi << 8));
  #pragma unroll
  for (int par = 0; par < 2; ++par) c.vksrc[par] = KRS * (lane >> 4) + 16u * ((lane & 15u) ^ (4u * par + (lane >> 4)));
  c.vvsrc = KRS * ((lane >> 2) & 7u) + 64u * hi + 16u * (lane & 3u);
  c.m0k = rfl(c.ldsb + KRING + 4096u * wid); c.m0v = rfl(c.ldsb + VRING + 4096u * wid);
  c.sofk = rfl(KRS * 16u * wid); c.sofv = c.sofk;
  const unsigned uw = rfl(wid);
  {
    const size_t kvoff = ((size_t)(b * 32u + h)) * (1024u * 256u);
    c.srdK = __builtin_amdgcn_make_buffer_rsrc((void*)((const char*)Kc + kvoff), (short)0, KVREC, SRD_WORD3);
    c.srdV = __builtin_amdgcn_make_buffer_rsrc((void*)((const char*)Vc + kvoff), (short)0, KVREC, SRD_WORD3);
    p.srdW = __builtin_amdgcn_make_buffer_rsrc((void*)((const char*)Wq + h * 256u), (short)0, WREC, SRD_WORD3);
    p.srdX = __builtin_amdgcn_make_buffer_rsrc((void*)((const char*)X + (size_t)(b * 256u + 64u * uw) * 1024u), (short)0, XREC, SRD_WORD3);
  }
  {
    unsigned vr[6];
    #pragma unroll
    for (int par = 0; par < 2; ++par) vr[par] = c.ldsb + OSTAGE + 8192u * wid + 128u * (lane >> 3) + 16u * ((lane & 7u) ^ ((4u * par + (lane >> 4)) & 7u));
    #pragma unroll
    for (int ks = 0; ks < 4; ++ks) vr[2 + ks] = c.ldsb + OSTAGE + 8192u * wid + 128u * l32 + 16u * ((2u * ks + hi) ^ ((l32 >> 1) & 7u));
    unsigned vwb0 = c.ldsb + (((lane & 3u) << 3) | (((lane >> 2) & 3u) << 6) | (((lane >> 4) & 1u) << 5) | (hi << 11)), vwb1 = vwb0 + 65536u;
    unsigned vws = WRS * ((lane >> 2) & 7u) + 64u * hi + 16u * (lane & 3u);
    unsigned vxs0 = 1024u * (lane >> 3) + 16u * (lane & 7u);
    unsigned vl16 = 16u * lane, vwo = 1024u * ((lane >> 2) & 7u) + 64u * hi + 16u * (lane & 3u);
    const unsigned m0w = rfl(c.ldsb + 4096u * wid), sofw = rfl(WRS * 16u * wid);
    const unsigned sofv2 = rfl(8192u * (wid >> 1) + 128u * (wid & 1u));
    unsigned koff = KTILEB * 4u + c.sofk, voff = KTILEB * 3u + sofv2;
    unsigned m0v2 = rfl(c.ldsb + VRING + 8192u * (wid >> 1) + 1024u * (wid & 1u)), m0k2 = c.m0k;
    unsigned w0 = c.vkb[0], w1 = c.vkb[1], w2 = c.vkb[2], w3 = c.vkb[3], w4 = c.vvb;
    const unsigned img = 2u * bx + (uw >> 1), wnt = img >> 6, wkt = img & 63u;
    __amdgpu_buffer_rsrc_t srdP = __builtin_amdgcn_make_buffer_rsrc((void*)((const char*)Wo + (size_t)(64u * wkt + 32u * (uw & 1u)) * 1024u + wnt * 128u), (short)0, 32768, SRD_WORD3);
    unsigned long long tsx = 0;
    asm volatile("s_nop 4\n\t" MAINLOOP_ASM
                 : "={v239}"(c.lsum[0]), "={v240}"(c.lsum[1]), [koff] "+s"(koff), [voff] "+s"(voff), [m0k] "+s"(m0k2), [m0v] "+s"(m0v2), [ts2] "+s"(tsx),
                   "+{v224}"(w0), "+{v225}"(w1), "+{v226}"(w2), "+{v227}"(w3), "+{v228}"(w4),
                   "+{v128}"(vwb0), "+{v129}"(vwb1), "+{v130}"(vws), "+{v131}"(vxs0), "+{v133}"(vl16), "+{v134}"(vwo),
                   "+{v136}"(vr[0]), "+{v137}"(vr[1]), "+{v138}"(vr[2]), "+{v139}"(vr[3]), "+{v140}"(vr[4]), "+{v141}"(vr[5])
                 : "{v229}"(c.vksrc[0]), "{v230}"(c.vksrc[1]), "{v231}"(c.vvsrc), "{v232}"(c.vvsrc + 4096u), [srdk] "s"(c.srdK), [srdv] "s"(c.srdV),
                   [srdx] "s"(p.srdX), [srdw] "s"(p.srdW), [srdp] "s"(srdP), [m0w] "s"(m0w), [sofw] "s"(sofw),
                   [sofk] "s"(c.sofk), [sofv] "s"(sofv2), [pfm0] "s"(rfl(c.ldsb + 5u * 16384u + 1024u * wid)), [pfoff] "s"(rfl((i5 * 4u + wid) * 1024u)),
                   [wom0] "s"(rfl(c.ldsb + OSTAGE + 8192u * wid))
                 : MAINLOOP_CLOB);
  }
  STAMP(4); STAMP(5);
  STAMP(6);
  {
    unsigned lb;
    asm volatile("v_mbcnt_lo_u32_b32 %0, -1, 0\n\tv_mbcnt_hi_u32_b32 %0, -1, %0" : "=v"(lb));
    {
      const unsigned tb = c.ldsb + OSTAGE + 8192u * uw + (((lb & 3u) << 3) | (((lb >> 2) & 3u) << 6) | (((lb >> 4) & 1u) << 5) | ((lb >> 5) << 10));
      u32x2 t[8];
      asm volatile("ds_read_b64_tr_b16 %0, %8\n\tds_read_b64_tr_b16 %1, %8 offset:256\n\tds_read_b64_tr_b16 %2, %8 offset:512\n\tds_read_b64_tr_b16 %3, %8 offset:768\n\t"
                   "ds_read_b64_tr_b16 %4, %8 offset:2048\n\tds_read_b64_tr_b16 %5, %8 offset:2304\n\tds_read_b64_tr_b16 %6, %8 offset:2560\n\tds_read_b64_tr_b16 %7, %8 offset:2816\n\ts_waitcnt lgkmcnt(0)"
                   : "=&v"(t[0]), "=&v"(t[1]), "=&v"(t[2]), "=&v"(t[3]), "=&v"(t[4]), "=&v"(t[5]), "=&v"(t[6]), "=&v"(t[7]) : "v"(tb) : "memory");
      const unsigned img = 2u * bx + (uw >> 1), wnt = img >> 6, wkt = img & 63u;
      #pragma unroll
      for (int ks = 0; ks < 2; ++ks)
        #pragma unroll
        for (int nb = 0; nb < 2; ++nb) {
          u32x4 w = {t[4 * ks + 2 * nb][0], t[4 * ks + 2 * nb][1], t[4 * ks + 2 * nb + 1][0], t[4 * ks + 2 * nb + 1][1]};
          char* dst = (char*)O + (16u << 20) + (size_t)((wnt * 2u + nb) * 256u + wkt * 4u + 2u * (uw & 1u) + ks) * 1024u;
          asm volatile("global_store_dwordx4 %0, %1, %2 " OSTPOL "\n\ts_nop 1" :: "v"(16u * lb), "v"(w), "s"(dst) : "memory");
        }
    }
    #pragma unroll
    for (int qb = 0; qb < 2; ++qb) {
      float il = __builtin_amdgcn_rcpf(c.lsum[qb]);
      asm volatile("s_nop 1" : "+v"(il));
      char* dst = (char*)O + (size_t)(((b * 4u + uw) * 2u + qb) * 256u + 8u * h) * 1024u;
      #pragma unroll
      for (int db = 0; db < 4; ++db) {
        float o[16];
        W4_READ_O(db, qb, o);
        #pragma unroll
        for (int sx = 0; sx < 2; ++sx) {
          const unsigned a0 = cvtpk(fmul_s(o[8 * sx + 0], il), fmul_s(o[8 * sx + 1], il)), a1 = cvtpk(fmul_s(o[8 * sx + 2], il), fmul_s(o[8 * sx + 3], il));
          const unsigned b0 = cvtpk(fmul_s(o[8 * sx + 4], il), fmul_s(o[8 * sx + 5], il)), b1 = cvtpk(fmul_s(o[8 * sx + 6], il), fmul_s(o[8 * sx + 7], il));
          auto r0 = __builtin_amdgcn_permlane32_swap(a0, b0, false, false); auto r1 = __builtin_amdgcn_permlane32_swap(a1, b1, false, false);
          u32x4 w = {r0[0], r1[0], r0[1], r1[1]};
          asm volatile("global_store_dwordx4 %0, %1, %2 offset:%c3 " OSTPOL "\n\ts_nop 1" :: "v"(16u * lb + 4096u * (db >> 1)), "v"(w), "s"(dst), "i"(1024 * (2 * (db & 1) + sx)) : "memory");
        }
      }
    }
  }
}

#define OFRAG 1
#define K2WREC 524288
#define K2OREC 524288
#define K2_ACLOB W4_ACLOB, W4_ACLOB_HI
struct OPC {
  f32x16 acc[2][2];
  unsigned v0, v1, so;
  __amdgpu_buffer_rsrc_t srdW, srdO;
};
template <int R> DI void op_load(OPC& p) {
  asm volatile("buffer_load_dwordx4 a[%c5:%c6], %1, %3, %0 offen\n\tbuffer_load_dwordx4 a[%c7:%c8], %2, %3, %0 offen\n\t"
               "buffer_load_dwordx4 a[%c9:%c10], %1, %4, %0 offen\n\tbuffer_load_dwordx4 a[%c11:%c12], %2, %4, %0 offen\n\ts_add_u32 %0, %0, 4096"
               : "+s"(p.so) : "v"(p.v0), "v"(p.v1), "s"(p.srdW), "s"(p.srdO), "i"(16 * R), "i"(16 * R + 3), "i"(16 * R + 4), "i"(16 * R + 7), "i"(16 * R + 8), "i"(16 * R + 11), "i"(16 * R + 12), "i"(16 * R + 15)
               : "memory", "scc", K2_ACLOB);
}
template <int R, bool TAIL> DI void op_step(OPC& p) {
  asm volatile("s_waitcnt vmcnt(%c12)\n\t"
               "v_mfma_f32_32x32x16_bf16 %0, a[%c4:%c5], a[%c8:%c9], %0\n\tv_mfma_f32_32x32x16_bf16 %1, a[%c4:%c5], a[%c10:%c11], %1\n\t"
               "v_mfma_f32_32x32x16_bf16 %2, a[%c6:%c7], a[%c8:%c9], %2\n\tv_mfma_f32_32x32x16_bf16 %3, a[%c6:%c7], a[%c10:%c11], %3"
               : "+v"(p.acc[0][0]), "+v"(p.acc[0][1]), "+v"(p.acc[1][0]), "+v"(p.acc[1][1])
               : "i"(16 * R), "i"(16 * R + 3), "i"(16 * R + 4), "i"(16 * R + 7), "i"(16 * R + 8), "i"(16 * R + 11), "i"(16 * R + 12), "i"(16 * R + 15),
                 "i"((TAIL && R > 2) ? 4 * (15 - R) : 52) : "memory", K2_ACLOB);
  if constexpr (!TAIL || R < 2) op_load<(R + 14) & 15>(p);
}
template <int... Rs> DI void op_loads(OPC& p, iseq<Rs...>) { (op_load<Rs>(p), ...); }
template <bool TAIL, int... Rs> DI void op_steps(OPC& p, iseq<Rs...>) { (op_step<Rs, TAIL>(p), ...); }

extern "C" __global__ __launch_bounds__(256, 1)
void oproj(const unsigned short* __restrict__ O, const unsigned short* __restrict__ Wo, unsigned short* __restrict__ out, char* __restrict__ ws) {
  extern __shared__ __attribute__((aligned(16))) char lds[];
  const unsigned tid = threadIdx.x, lane = tid & 63u, wid = tid >> 6, l32 = lane & 31u, hi = lane >> 5;
  const unsigned bx = blockIdx.x, xcd = bx & 7u, i5 = bx >> 3;
  const unsigned mt = xcd * 4u + (i5 >> 3), nt = i5 & 7u;
  const unsigned uw = rfl(wid);
  STAMP(0);
  OPC p;
  p.srdW = __builtin_amdgcn_make_buffer_rsrc((void*)(ws + (16u << 20) + (size_t)nt * 524288u), (short)0, K2WREC, SRD_WORD3);
  p.srdO = __builtin_amdgcn_make_buffer_rsrc((void*)(ws + (size_t)mt * 524288u), (short)0, K2OREC, SRD_WORD3);
  p.v0 = 16u * lane + 1024u * wid; p.v1 = p.v0 + 262144u; p.so = 0u;
  #pragma unroll
  for (int i = 0; i < 4; ++i) p.acc[i >> 1][i & 1] = f32x16{};
  op_loads(p, make_iseq<14>{});
  STAMP(1);
  for (int it = 0; it < 3; ++it) op_steps<false>(p, make_iseq<16>{});
  op_steps<true>(p, make_iseq<16>{});
  STAMP(2);
  asm volatile("s_waitcnt vmcnt(0)\n\ts_nop 7\n\ts_nop 7" : "+v"(p.acc[0][0]), "+v"(p.acc[0][1]), "+v"(p.acc[1][0]), "+v"(p.acc[1][1]) :: "memory");
  #pragma unroll
  for (int d = 0; d < 4; ++d)
    #pragma unroll
    for (int q = 0; q < 4; ++q) {
      const f32x16& a = p.acc[d >> 1][d & 1];
      f32x4 v = {a[4 * q], a[4 * q + 1], a[4 * q + 2], a[4 * q + 3]};
      *(f32x4*)(lds + 16384u * uw + 4096u * d + 1024u * q + 16u * lane) = v;
    }
  __syncthreads();
  float r[16];
  #pragma unroll
  for (int q = 0; q < 4; ++q) {
    f32x4 t = *(const f32x4*)(lds + 4096u * uw + 1024u * q + 16u * lane);
    #pragma unroll
    for (int s2 = 1; s2 < 4; ++s2) t += *(const f32x4*)(lds + 16384u * s2 + 4096u * uw + 1024u * q + 16u * lane);
    r[4 * q] = t[0]; r[4 * q + 1] = t[1]; r[4 * q + 2] = t[2]; r[4 * q + 3] = t[3];
  }
  const unsigned m = mt * 64u + (uw & 1u) * 32u + l32, n0 = nt * 64u + (uw >> 1) * 32u + 4u * hi;
  #pragma unroll
  for (int g = 0; g < 4; ++g) {
    u32x2 w = {cvtpk(r[4 * g], r[4 * g + 1]), cvtpk(r[4 * g + 2], r[4 * g + 3])};
    *(u32x2*)(out + (size_t)m * 512u + n0 + 8u * g) = w;
  }
  asm volatile("k2_code_end:" ::: "memory");
}

extern "C" void kernel_launch(void* const* d_in, const int* in_sizes, int n_in, void* d_out, int out_size, void* d_ws, size_t ws_size, hipStream_t stream) {
  hipFuncSetAttribute((const void*)attn_fused, hipFuncAttributeMaxDynamicSharedMemorySize, (int)SHM_ATTN);
  hipFuncSetAttribute((const void*)oproj, hipFuncAttributeMaxDynamicSharedMemorySize, 65536);
  unsigned short* o = (unsigned short*)d_ws;
  attn_fused<<<256, 256, SHM_ATTN, stream>>>((const unsigned short*)d_in[0], (const unsigned short*)d_in[1], (const unsigned short*)d_in[2], (const unsigned short*)d_in[3], o, (const unsigned short*)d_in[4]);
  oproj<<<256, 256, 65536, stream>>>(o, (const unsigned short*)d_in[4], (unsigned short*)d_out, (char*)d_ws);
}
```
